```python
import torch

def _softmax(x, axis=-1):
    return torch.softmax(x, dim=axis)

def _gelu(x):
    return torch.nn.functional.gelu(x, approximate='tanh')

def _silu(x):
    return torch.nn.functional.silu(x)

def _sigmoid(x):
    return torch.sigmoid(x)

def _layernorm(x, axis=-1, eps=1e-05):
    m = x.mean(dim=axis, keepdim=True)
    v = x.var(dim=axis, keepdim=True, unbiased=False)
    return (x - m) / torch.sqrt(v + eps)

def _scatter_add(tgt, idx, src, axis=0):
    return tgt.clone().index_add_(axis, idx, src)

def _segment_sum(src, seg, n):
    out = torch.zeros((n,) + src.shape[1:], dtype=src.dtype)
    return out.index_add_(0, seg, src)

def _pad(x, pw):
    flat = tuple((v for pair in reversed(pw) for v in pair))
    return torch.nn.functional.pad(x, flat)
kernel_name = 'concat_heads__memory_aug_recurrent_mem_tr__sliding_block__prompt'

def setup_inputs():

    _w = lambda a: (lambda t: t.to(torch.bfloat16) if t.is_floating_point() else t)(torch.as_tensor(a))

    def _A_setup():
        B, S, Dm = (1, 2048, 1024)
        H, Dh = (32, 128)
        M, seg = (8, 32)
        seg = min(seg, S)
        S = S // seg * seg
        return {'x': _w((lambda *s: torch.randn(*s, dtype=torch.bfloat16) * 0.05)(B, S, Dm)), 'mem0': _w((lambda *s: torch.randn(*s, dtype=torch.bfloat16) * 1.6)(B, M, Dm)), 'Wq': _w((lambda *s: torch.randn(*s, dtype=torch.bfloat16) * 0.8)(Dm, H, Dh) * Dm ** (-0.5)), 'Wk': _w((lambda *s: torch.randn(*s, dtype=torch.bfloat16) * 0.8)(Dm, H, Dh) * Dm ** (-0.5)), 'Wv': _w((lambda *s: torch.randn(*s, dtype=torch.bfloat16) * 1.6)(Dm, H, Dh) * Dm ** (-0.5)), 'Wo': _w((lambda *s: torch.randn(*s, dtype=torch.bfloat16) * 1.6)(H, Dh, Dm) * (H * Dh) ** (-0.5)), 'seg_len': seg}
    _A_in = _A_setup()

    def _B_setup():
        B, S, D = (1, 2048, 1024)
        return {'x': _w((lambda *s: torch.randn(*s, dtype=torch.bfloat16) * 0.2)(B, S, D)), 'wqkv': _w((lambda *s: torch.randn(*s, dtype=torch.bfloat16) * 0.2)(D, 3 * D)), 'wo': _w((lambda *s: torch.randn(*s, dtype=torch.bfloat16) * 0.05)(D, D))}
    _B_in = _B_setup()
    return {'A_x': _A_in['x'], 'A_mem0': _A_in['mem0'], 'A_Wq': _A_in['Wq'], 'A_Wk': _A_in['Wk'], 'A_Wv': _A_in['Wv'], 'A_Wo': _A_in['Wo'], 'A_seg_len': _A_in['seg_len'], 'B_x': _B_in['x'], 'B_wqkv': _B_in['wqkv'], 'B_wo': _B_in['wo']}

def reference(A_x, A_mem0, A_Wq, A_Wk, A_Wv, A_Wo, A_seg_len, B_x, B_wqkv, B_wo):

    def _A_ref(x, mem0, Wq, Wk, Wv, Wo, seg_len):
        B, S, Dm = x.shape
        H, Dh = (Wq.shape[1], Wq.shape[2])
        M = mem0.shape[1]
        scale = Dh ** (-0.5)
        n_seg = S // seg_len
        L = M + seg_len
        idx = torch.arange(L)
        mask = (idx[:, None] < M) | (idx[:, None] >= idx[None, :])
        mem = mem0
        outs = []
        for s in range(n_seg):
            xs = x[:, s * seg_len:(s + 1) * seg_len, :].clone()
            h = torch.concatenate([mem, xs], axis=1)
            q = torch.einsum('btd,dhe->bhte', h, Wq)
            k = torch.einsum('btd,dhe->bhte', h, Wk)
            v = torch.einsum('btd,dhe->bhte', h, Wv)
            scores = torch.einsum('bhqe,bhke->bhqk', q, k) * scale
            scores = torch.where(mask[None, None], scores, -1000000000.0)
            p = _softmax(scores, axis=-1)
            o = torch.einsum('bhqk,bhke->bhqe', p, v)
            o = torch.einsum('bhte,hed->btd', o, Wo)
            mem = o[:, :M, :].clone()
            outs.append(o[:, M:, :])
        return torch.concatenate(outs, axis=1)

    def _B_ref(x, wqkv, wo):
        B, S, D = x.shape
        H = 32
        Dh = D // H
        W = 256
        qkv = (lambda x, *d: x.permute(*d))((x @ wqkv).reshape(B, S, 3, H, Dh), 2, 0, 3, 1, 4)
        q, k, v = (qkv[0], qkv[1], qkv[2])
        scores = torch.einsum('bhqd,bhkd->bhqk', q, k) * Dh ** (-0.5)
        idx = torch.arange(S)
        mask = (idx[:, None] - idx[None, :] >= 0) & (idx[:, None] - idx[None, :] < W)
        scores = torch.where(mask[None, None], scores, -1000000000.0)
        p = _softmax(scores, axis=-1)
        o = torch.einsum('bhqk,bhkd->bhqd', p, v)
        return (lambda x, *d: x.permute(*d))(o, 0, 2, 1, 3).reshape(B, S, D) @ wo
    a = _A_ref(A_x, A_mem0, A_Wq, A_Wk, A_Wv, A_Wo, A_seg_len)
    b = _B_ref(B_x, B_wqkv, B_wo)
    return torch.concatenate([a, b], axis=-1)
```

```cpp
#include <hip/hip_runtime.h>
#include <hip/hip_bf16.h>

#define LAS __attribute__((address_space(3)))
typedef unsigned u32;
typedef unsigned short u16;
typedef u32 u32x2 __attribute__((ext_vector_type(2)));
typedef u32 u32x4 __attribute__((ext_vector_type(4)));
typedef u32 u32x8 __attribute__((ext_vector_type(8)));
typedef float f32x4 __attribute__((ext_vector_type(4)));
typedef short s16x4 __attribute__((ext_vector_type(4)));
typedef __bf16 bf16x2 __attribute__((ext_vector_type(2)));
typedef __bf16 bf16x8 __attribute__((ext_vector_type(8)));
typedef __bf16 bf16x16 __attribute__((ext_vector_type(16)));
typedef __amdgpu_buffer_rsrc_t rsrc_t;

constexpr int NSEG = 64;
constexpr u32 SENT = 0xFFFFFFFFu;
constexpr int SC1 = 16;
constexpr u32 CTRL_OFF = 0, CTRL_BYTES = 0xC000;
constexpr u32 TMO_OFF = 0;
constexpr u32 TS0_OFF = 0x400;
constexpr u32 XF_OFF = 0x1000;
constexpr u32 OAC_OFF = 0x2000;
constexpr u32 WF_OFF = 0x3000;
constexpr u32 BF_OFF = 0x4000;
constexpr u32 OBC_OFF = 0x4800;
constexpr u32 RDY_OFF = 0x8000;
constexpr u32 MX_OFF = 0x10000, MX_SLOT = 0x5000, MX_TS = 16384;
constexpr u32 QX_OFF = 0x40000, QX_SLOT = 32 * 8192;
constexpr u32 PX_OFF = 0x200000, PX_SLOT = 128 * 8192;
constexpr u32 KXF_OFF = 0x1000000, VXF_OFF = 0x2000000, QXF_OFF = 0x3000000, OAF_OFF = 0x4000000;
constexpr u32 QB_OFF = 0x5000000, KB_OFF = 0x5400000, VB_OFF = 0x5800000, OB_OFF = 0x5C00000;
constexpr u32 WP_OFF = 0x6000000;
constexpr u32 DBG_OFF = 0x7000000;
constexpr int L_M = 0, L_TS = 16384, L_QK = 17408, L_VM = L_QK + 4096, L_KX = L_VM + 4096, L_VX = L_KX + 8192, L_QX = L_VX + 8192, L_XB = 20480;
constexpr int L_STG = 0;
constexpr int LDS_BYTES = 147456;

__device__ __forceinline__ u32x4 ldg(rsrc_t r, u32 vo, u32 so) { return __builtin_amdgcn_raw_buffer_load_b128(r, vo, so, SC1); }
__device__ __forceinline__ u32x2 ldg64(rsrc_t r, u32 vo, u32 so) { return __builtin_amdgcn_raw_buffer_load_b64(r, vo, so, SC1); }
__device__ __forceinline__ u32 ldg32(rsrc_t r, u32 vo, u32 so) { return __builtin_amdgcn_raw_buffer_load_b32(r, vo, so, SC1); }
__device__ __forceinline__ void stg128(rsrc_t r, u32 vo, u32 so, u32x4 v) {
    asm volatile("" : "+v"(v));
    __builtin_amdgcn_raw_buffer_store_b128(v, r, vo, so, SC1);
    asm volatile("s_nop 1" :: "v"(v) : "memory");
}
__device__ __forceinline__ void stg32(rsrc_t r, u32 vo, u32 so, u32 v) { __builtin_amdgcn_raw_buffer_store_b32(v, r, vo, so, SC1); }
__device__ __forceinline__ u32x2 trrd(LAS char* p) { return __builtin_bit_cast(u32x2, __builtin_amdgcn_ds_read_tr16_b64_v4i16((LAS s16x4*)p)); }
__device__ __forceinline__ f32x4 mfma32(u32x4 a, u32x4 b, f32x4 c) { return __builtin_amdgcn_mfma_f32_16x16x32_bf16(__builtin_bit_cast(bf16x8, a), __builtin_bit_cast(bf16x8, b), c, 0, 0, 0); }
__device__ __forceinline__ f32x4 mfma16(u32x2 a, u32x2 b, f32x4 c) { return __builtin_amdgcn_mfma_f32_16x16x16bf16_1k(__builtin_bit_cast(s16x4, a), __builtin_bit_cast(s16x4, b), c, 0, 0, 0); }
__device__ __forceinline__ f32x4 smf(u32x4 a, u32x8 b, f32x4 c, int idx) { return __builtin_amdgcn_smfmac_f32_16x16x64_bf16(__builtin_bit_cast(bf16x8, a), __builtin_bit_cast(bf16x16, b), c, idx, 0, 0); }
__device__ __forceinline__ u32 pk(float lo, float hi) { bf16x2 v = {(__bf16)lo, (__bf16)hi}; return __builtin_bit_cast(u32, v); }
__device__ __forceinline__ u32 fb(float f) { return __builtin_bit_cast(u32, f); }
__device__ __forceinline__ float bf(u32 u) { return __builtin_bit_cast(float, u); }
__device__ __forceinline__ u32 umax4(u32x4 v) { return max(max(v.x, v.y), max(v.z, v.w)); }
#define LBAR() asm volatile("s_waitcnt lgkmcnt(0)\n\ts_barrier" ::: "memory")
#define MEMFENCE() asm volatile("" ::: "memory")
__device__ __forceinline__ void nap(int n) { for (int i = 0; i < n; ++i) __builtin_amdgcn_s_sleep(1); }
__device__ __forceinline__ float hadd32(float a, float b) { auto r = __builtin_amdgcn_permlane32_swap(fb(a), fb(b), false, false); return bf(r[0]) + bf(r[1]); }
constexpr u32 SPIN_CAP = 1u << 15;

struct Args { const u16* Ax; const u16* mem0; const u16* Wq; const u16* Wk; const u16* Wv; const u16* Wo; const u16* Bx; const u16* wqkv; const u16* wo; u16* out; char* ws; int d1, d2, d2v, d3, m1, m2, m2v, m3, flags, slack, pa, pad; };

__device__ __forceinline__ void give_up(rsrc_t ws, u32 code) { stg32(ws, 0, TMO_OFF, code); }
__device__ __forceinline__ float mx3(float a, float b, float c) { return __builtin_fmaxf(__builtin_fmaxf(a, b), c); }
__device__ __forceinline__ float mx2v(float a, float b) { float r; asm("v_max_f32 %0, %1, %2" : "=v"(r) : "v"(a), "v"(b)); return r; }
#define SCHED() __builtin_amdgcn_sched_barrier(0)
#define PIN(x) asm volatile("" : "+v"(x))
__device__ __forceinline__ float rowmax(const f32x4& a, const f32x4& b, const f32x4& c) {
    float m = mx3(mx3(mx3(a[0], a[1], a[2]), a[3], b[0]), mx3(b[1], b[2], b[3]), mx3(mx3(c[0], c[1], c[2]), c[3], a[3]));
    { auto r = __builtin_amdgcn_permlane32_swap(fb(m), fb(m), false, false); m = mx2v(bf(r[0]), bf(r[1])); }
    { auto r = __builtin_amdgcn_permlane16_swap(fb(m), fb(m), false, false); m = mx2v(bf(r[0]), bf(r[1])); }
    return m;
}
__device__ __forceinline__ u32 now32() { return (u32)__builtin_amdgcn_s_memrealtime(); }
__device__ __forceinline__ void wait_until(u32 t) { while ((int)(now32() - t) < 0) {} }
__device__ __forceinline__ bool wait_sched(u32 t, int m) { const int d = (int)(t - now32()); nap((max(d, m) * 23) >> 6); return d < m; }
__device__ __forceinline__ u32 anchor(LAS char* p, u32 base, int xb, int slack, bool& late) {
    const u32x4 a = *(LAS u32x4*)p, b = *(LAS u32x4*)(p + 16);
    const u32 n = now32();
    const int m = max(max(max((int)(a.x - base), (int)(a.y - base)), max((int)(a.z - base), (int)(a.w - base))), max(max((int)(b.x - base), (int)(b.y - base)), max((int)(b.z - base), (int)(b.w - base))));
    const u32 f = (a.x | a.y | a.z | a.w | b.x | b.y | b.z | b.w) & 1;
    u32 t = base + (u32)__builtin_amdgcn_readfirstlane(m + xb + (f ? (slack & 255) : 0));
    late = (int)(n - t) > 0;
    return late ? n + (slack >> 8) : t;
}
#define TS(k)
#define TSDECL
#define TSUM(n)
#define TSOUT
#define FAIL(k)

__device__ __forceinline__ void poll_words(rsrc_t ws, u32 off, int n, int lane, u32& cap, u32 code) {
    for (u32 sp = 0;; ++sp) {
        MEMFENCE();
        const u32 v = ldg32(ws, min(lane, n - 1) * 4, off);
        if (__all(v != SENT)) break;
        if (sp > cap) { give_up(ws, code); cap = 0; break; }
        __builtin_amdgcn_s_sleep(4);
    }
}
__device__ __forceinline__ void poll_count(rsrc_t ws, u32 off, u32 want, u32& cap, u32 code) {
    for (u32 sp = 0;; ++sp) {
        MEMFENCE();
        const u32 v = __builtin_amdgcn_readfirstlane(ldg32(ws, 0, off));
        if (v == want) break;
        if (sp > cap) { give_up(ws, code); cap = 0; break; }
        __builtin_amdgcn_s_sleep(4);
    }
}
__device__ __forceinline__ void count_up(char* wsp, u32 off) { __hip_atomic_fetch_add((__attribute__((address_space(1))) u32*)(wsp + off), 1u, __ATOMIC_RELAXED, __HIP_MEMORY_SCOPE_AGENT); }
#define DRAIN() asm volatile("s_waitcnt vmcnt(0)" ::: "memory")

__device__ __forceinline__ void chain_role(const Args& a, LAS char* L, const rsrc_t ws, const int cu) {
    const int tid = threadIdx.x, lane = tid & 63, w = __builtin_amdgcn_readfirstlane(tid >> 6);
    const int h = cu & 31, j = cu >> 5;
    const int g = lane >> 4, l16 = lane & 15;
    const u32x4 sent4 = {SENT, SENT, SENT, SENT};
    u32 cap = SPIN_CAP;
    TSDECL;
#pragma unroll
    for (int sl = 0; sl < 4; ++sl) stg128(ws, tid * 16, PX_OFF + sl * PX_SLOT + cu * 8192, sent4);
    { const int sl = tid >> 7, t = tid & 127; const u32 o = t < 32 ? j * 512 + t * 16 : (t < 64 ? 2048 + j * 512 + (t - 32) * 16 : 4096 + j * 1024 + (t - 64) * 16);
      stg128(ws, sl * QX_SLOT + o, QX_OFF + h * 8192, sent4); }
    if (tid < 32) stg128(ws, (tid >> 3) * MX_SLOT + (tid & 7) * 16, MX_OFF + cu * 128, sent4);
    const bool rep = (cu & 15) == 0;
    if (rep && tid < 4) stg32(ws, tid * MX_SLOT, MX_OFF + MX_TS + (cu >> 4) * 128, SENT);
    auto stage1 = [&]() {
#pragma unroll
        for (int i = 0; i < 16; ++i) {
            const int piece = i * 8 + w;
            const int mat = piece >> 6, row = (piece & 63) * 16 + (lane >> 2);
            const u16* src = (mat ? a.Wk : a.Wq) + (size_t)row * 4096 + h * 128 + j * 32 + (lane & 3) * 8;
            __builtin_amdgcn_global_load_lds((const u32*)src, (LAS u32*)(L + piece * 1024), 16, 0, 0);
        }
        asm volatile("s_waitcnt vmcnt(0)" ::: "memory");
        LBAR();
        if (tid == 0) stg32(ws, 0, RDY_OFF + cu * 128, 0u);
    };
    auto stage2 = [&]() {
        LBAR();
#pragma unroll
        for (int i = 0; i < 16; ++i) {
            const int piece = i * 8 + w;
            const u16* src = piece < 64 ? a.Wv + (size_t)(piece * 16 + (lane >> 2)) * 4096 + h * 128 + j * 32 + (lane & 3) * 8
                                        : a.Wo + (size_t)(h * 128 + (piece - 64) * 2 + (lane >> 5)) * 1024 + j * 256 + (lane & 31) * 8;
            __builtin_amdgcn_global_load_lds((const u32*)src, (LAS u32*)(L + piece * 1024), 16, 0, 0);
        }
        asm volatile("s_waitcnt vmcnt(0)" ::: "memory");
        LBAR();
    };
    const int qt = j & 1, eh = j >> 1;
    auto stage3 = [&]() {
        LBAR();
        u32 c[8];
#pragma unroll
        for (int r = 0; r < 8; ++r) c[r] = *(const u32*)(a.mem0 + r * 1024 + tid * 2);
        u32x4 lo = {(c[0] & 0xffff) | (c[1] << 16), (c[2] & 0xffff) | (c[3] << 16), (c[4] & 0xffff) | (c[5] << 16), (c[6] & 0xffff) | (c[7] << 16)};
        u32x4 hi = {(c[0] >> 16) | (c[1] & 0xffff0000u), (c[2] >> 16) | (c[3] & 0xffff0000u), (c[4] >> 16) | (c[5] & 0xffff0000u), (c[6] >> 16) | (c[7] & 0xffff0000u)};
        *(LAS u32x4*)(L + L_M + tid * 32) = lo; *(LAS u32x4*)(L + L_M + tid * 32 + 16) = hi;
        if (!(a.flags & 4)) poll_words(ws, XF_OFF + h * 128, 3, lane, cap, 2);
        const u32x4 kx = ldg(ws, tid * 16, KXF_OFF + h * 8192), vx = ldg(ws, tid * 16, VXF_OFF + h * 8192);
        *(LAS u32x4*)(L + L_KX + tid * 16) = kx; *(LAS u32x4*)(L + L_VX + tid * 16) = vx;
        if (w < 4) *(LAS u32x4*)(L + L_QX + tid * 16) = ldg(ws, tid * 16, QXF_OFF + h * 8192 + qt * 4096);
        LBAR();
        if (w == 7) {
            if (rep && lane == 0) stg32(ws, 0, TS0_OFF + (cu >> 4) * 128, min(now32() | 1u, SENT - 2));
            for (u32 sp = 0;; ++sp) {
                MEMFENCE();
                const u32 r0 = ldg32(ws, lane * 128, RDY_OFF), r1 = ldg32(ws, lane * 128, RDY_OFF + 8192), t = ldg32(ws, (lane & 7) * 128, TS0_OFF);
                *(LAS u32*)(L + L_TS + lane * 4) = t;
                if (__all(r0 != SENT && r1 != SENT && t != SENT)) break;
                if (sp > cap) { give_up(ws, 1); cap = 0; break; }
                __builtin_amdgcn_s_sleep(8);
            }
        }
        LBAR();
    };
    const int idx = (l16 & 8) ? 0xEEEE : 0x4444;
    const f32x4 z4 = {0.f, 0.f, 0.f, 0.f};
    constexpr float SCL2 = 0.08838834764831845f * 1.4426950408889634f;
    u32 tref = now32();
    u32 l16b = lane * 16;
    PIN(l16b);
    LAS char* const mwr = L + L_M + w * 2048 + l16b;
    auto hop1 = [&](int s) {
        const u32 off = MX_OFF + (s & 3) * MX_SLOT + w * 2048;
        wait_sched(tref + a.d1, a.m1);
        for (u32 sp = 0;; ++sp) {
            MEMFENCE();
            const u32x4 v0 = ldg(ws, l16b, off), v1 = ldg(ws, l16b + 1024, off);
            u32 m = 0;
            if (w == 7) { m = ldg32(ws, (l16b & 112) * 8, MX_OFF + (s & 3) * MX_SLOT + MX_TS); *(LAS u32*)(L + L_TS + (l16b >> 2)) = m; }
            *(LAS u32x4*)(mwr) = v0; *(LAS u32x4*)(mwr + 1024) = v1;
            if (!__any(max(m, max(umax4(v0), umax4(v1))) == SENT)) break;
            FAIL(10);
            if (sp > cap) { give_up(ws, 0x100 + s); cap = 0; break; }
        }
    };
    auto rd = [&](LAS char* p) { const u32x2 x0 = trrd(p), x1 = trrd(p + 256); return u32x4{x0.x, x0.y, x1.x, x1.y}; };

    if (w < 4) {
        u32x8 wq[16], wv[8];
        const int t = w & 1, kh = w >> 1;
        stage1();
        {
            LAS char* base = L + (w >> 1) * 65536 + (16 * (g & 1) + 8 * (g >> 1) + (l16 >> 2)) * 64 + t * 32 + (l16 & 3) * 8;
            LAS char* bA = base + (1 - kh) * 32768, *bB = base + kh * 32768;
#pragma unroll
            for (int ks = 0; ks < 16; ++ks)
#pragma unroll
                for (int q = 0; q < 4; ++q) { const u32x2 v = trrd((ks < 8 ? bA : bB) + (64 * (ks & 7) + 32 * (q >> 1) + 4 * (q & 1)) * 64); wq[ks][2 * q] = v.x; wq[ks][2 * q + 1] = v.y; }
        }
        stage2();
        {
            LAS char* base = L + (512 * kh + 16 * (g & 1) + 8 * (g >> 1) + (l16 >> 2)) * 64 + t * 32 + (l16 & 3) * 8;
#pragma unroll
            for (int ks = 0; ks < 8; ++ks)
#pragma unroll
                for (int q = 0; q < 4; ++q) { const u32x2 v = trrd(base + (64 * ks + 32 * (q >> 1) + 4 * (q & 1)) * 64); wv[ks][2 * q] = v.x; wv[ks][2 * q + 1] = v.y; }
        }
        stage3();
        LAS char* const mrd = L + L_M + (32 * (g >> 1) + 8 * (g & 1) + 4 * (l16 >> 3) + 2 * ((l16 >> 1) & 1) + ((l16 >> 2) & 1)) * 16 + (l16 & 1) * 8;
        LAS char* const mA = mrd + (1 - kh) * 8192, * const mB = mrd + kh * 8192;
        const u32 pub = (w >> 1) * 2048 + (32 * j + 16 * t + l16) * 16 + (g & 1) * 8 + (lane >> 5) * 4;
        const u32 pubv = 4096 + j * 1024 + ((2 * kh + (g & 1)) * 16 + l16) * 16 + t * 8 + (lane >> 5) * 4;
#pragma unroll 1
        for (int s = 0; s < NSEG; ++s) {
            const u32 qx = QX_OFF + (s & 3) * QX_SLOT + h * 8192;
            TS(0);
            if (s > 0) hop1(s);
            f32x4 c0 = z4, e0 = z4;
            PIN(c0); PIN(e0);
            TS(1);
            LBAR();
            TS(2);
            u32x4 af[8];
#pragma unroll
            for (int ks = 0; ks < 7; ++ks) af[ks] = rd(mA + ks * 1024);
            SCHED();
#pragma unroll
            for (int ks = 0; ks < 16; ++ks) {
                const int nx = ks + 7;
                c0 = smf(af[ks & 7], wq[ks], c0, idx);
                if (nx < 16) af[nx & 7] = nx < 8 ? rd(mA + nx * 1024) : rd(mB + (nx - 8) * 1024);
                SCHED();
            }
            TS(3);
            e0 = smf(af[0], wv[0], e0, idx); e0 = smf(af[1], wv[1], e0, idx);
            SCHED();
            stg32(ws, pub, qx, min(pk(hadd32(c0[0], c0[2]), hadd32(c0[1], c0[3])), SENT - 1));
            SCHED();
            TS(4);
#pragma unroll
            for (int ks = 2; ks < 8; ++ks) e0 = smf(af[ks], wv[ks], e0, idx);
            stg32(ws, pubv, qx, min(pk(hadd32(e0[0], e0[2]), hadd32(e0[1], e0[3])), SENT - 1));
            TS(5);
            bool late; tref = anchor(L + L_TS, tref, a.d2v + a.pa, a.slack, late) - a.pa;
            wait_sched(tref + a.d2, a.m2);
            TS(6);
            u32x4 qk = ldg(ws, l16b, qx + w * 1024);
            nap(a.m2v);
            u32x4 vv = ldg(ws, l16b, qx + 4096 + w * 1024);
            SCHED();
            for (u32 sp = 0;; ++sp) {
                *(LAS u32x4*)(mwr + (L_QK - L_M) - w * 1024) = qk;
                if (__builtin_expect(!__any(umax4(qk) == SENT), 1)) break;
                FAIL(11);
                if (sp > cap) { give_up(ws, 0x200 + s); cap = 0; break; }
                MEMFENCE();
                qk = ldg(ws, l16b, qx + w * 1024);
            }
            TS(7);
            LBAR();
            TS(8);
            for (u32 sp = 0;; ++sp) {
                *(LAS u32x4*)(mwr + (L_VM - L_M) - w * 1024) = vv;
                if (__builtin_expect(!__any(umax4(vv) == SENT), 1)) break;
                FAIL(12);
                if (sp > cap) { give_up(ws, 0x300 + s); cap = 0; break; }
                MEMFENCE();
                vv = ldg(ws, l16b, qx + 4096 + w * 1024);
            }
            LBAR();
            u32x4 v[5];
            {
                const int sn = min(s + 1, NSEG - 1);
                if ((sn & 7) == 0 && !(a.flags & 4)) poll_words(ws, XF_OFF + h * 128 + (sn >> 3) * 12, 3, lane, cap, 0x600 + s);
                const u32 src = (sn * 32 + h) * 8192;
                v[0] = ldg(ws, l16b, KXF_OFF + src + w * 2048); v[1] = ldg(ws, l16b + 1024, KXF_OFF + src + w * 2048); v[2] = ldg(ws, l16b, VXF_OFF + src + w * 2048); v[3] = ldg(ws, l16b + 1024, VXF_OFF + src + w * 2048);
                v[4] = ldg(ws, l16b, QXF_OFF + src + qt * 4096 + w * 1024);
            }
            if (s > 0) {
                const int sl = (s - 1) & 3;
                u32x4 sn = sent4; PIN(sn);
                stg128(ws, l16b, PX_OFF + sl * PX_SLOT + cu * 8192 + w * 2048, sn);
                stg128(ws, l16b + 1024, PX_OFF + sl * PX_SLOT + cu * 8192 + w * 2048, sn);
                const u32 qb = QX_OFF + sl * QX_SLOT + h * 8192;
                if (w < 2) { if (l16b < 512) stg128(ws, l16b, qb + w * 2048 + j * 512, sn); }
                else if (w == 2) stg128(ws, l16b, qb + 4096 + j * 1024, sn);
                else { if (l16b < 128) stg128(ws, l16b, MX_OFF + sl * MX_SLOT + cu * 128, sn); if (rep && l16b == 0) stg32(ws, 0, MX_OFF + sl * MX_SLOT + MX_TS + (cu >> 4) * 128, sn.x); }
            }
            {
                LAS char* dst = mwr - L_M + ((s + 1) & 1) * L_XB;
                *(LAS u32x4*)(dst + L_KX) = v[0]; *(LAS u32x4*)(dst + L_KX + 1024) = v[1]; *(LAS u32x4*)(dst + L_VX) = v[2]; *(LAS u32x4*)(dst + L_VX + 1024) = v[3];
                *(LAS u32x4*)(dst + L_QX - w * 1024) = v[4];
            }
            LBAR();
            TS(9); TSUM(10);
        }
    } else {
        const int ww = w - 4;
        u32x8 wo[8];
        stage1();
        stage2();
        {
            const int jj = l16 >> 2;
            LAS char* b2 = L + 65536 + (16 * (jj >> 1) + 32 * (g & 1) + 4 * (g >> 1) + (jj & 1)) * 512 + ww * 128 + (l16 & 3) * 8;
#pragma unroll
            for (int ct = 0; ct < 4; ++ct)
#pragma unroll
                for (int ks = 0; ks < 2; ++ks)
#pragma unroll
                    for (int m = 0; m < 4; ++m) { const u32x2 v = trrd(b2 + (64 * ks + 8 * (m >> 1) + 2 * (m & 1)) * 512 + ct * 32); wo[ct * 2 + ks][2 * m] = v.x; wo[ct * 2 + ks][2 * m + 1] = v.y; }
        }
        stage3();
        __builtin_amdgcn_s_setprio(2);
        LAS char* const qrd = L + L_QK + (4 * g + (l16 >> 2)) * 16 + (l16 & 1) * 8;
        const u32 pubp = (32 * j + 8 * ww + 2 * (lane >> 5) + (l16 >> 3)) * 8192 + h * 256 + ((l16 & 7) * 2 + (g & 1)) * 16;
        const u32 pubm = lane == 16 ? MX_TS + (cu >> 4) * 128 : cu * 128 + l16 * 8 + (lane >> 5) * 4;
        const bool pubm_on = !(lane & 16) || (rep && lane == 16);
        u32 mlo = (l16 & 8) ? 0u : ~0u;
        u32x4 halves = {0x3F003F00u, 0x3F003F00u, 0x3F003F00u, 0x3F003F00u};
        PIN(halves); PIN(mlo);
#pragma unroll 1
        for (int s = 0; s < NSEG; ++s) {
            const u32 qx = QX_OFF + (s & 3) * QX_SLOT + h * 8192;
            const u32 px_ = PX_OFF + (s & 3) * PX_SLOT;
            LAS char* const xb = L + (s & 1) * L_XB + l16b;
            u32x4 kx[8], vx[8];
            TS(0);
#pragma unroll
            for (int k = 0; k < 8; ++k) kx[k] = *(LAS u32x4*)(xb + L_KX + k * 1024);
#pragma unroll
            for (int k = 0; k < 8; ++k) vx[k] = *(LAS u32x4*)(xb + L_VX + k * 1024);
            if (s > 0) hop1(s);
            LBAR();
            bool late; tref = anchor(L + L_TS, tref, a.d2v, a.slack, late);
            f32x4 d[4] = {z4, z4, z4, z4};
            PIN(d[0]); PIN(d[1]); PIN(d[2]); PIN(d[3]);
            TS(1);
            LBAR();
            TS(2);
            u32x4 qf[4], km[4];
            qf[0] = rd(qrd); qf[1] = rd(qrd + 512); km[0] = rd(qrd + 2048); qf[2] = rd(qrd + 1024); km[1] = rd(qrd + 2048 + 512); qf[3] = rd(qrd + 1536); km[2] = rd(qrd + 2048 + 1024);
            SCHED();
            km[3] = rd(qrd + 2048 + 1536);
            u32x4 r0, r1, vm[4];
            {
                f32x4 sm, s0, s1;
                s0 = mfma32(kx[0], qf[0], z4); s1 = mfma32(kx[4], qf[0], z4); SCHED();
                s0 = mfma32(kx[1], qf[1], s0); s1 = mfma32(kx[5], qf[1], s1); sm = mfma32(km[0], qf[0], z4); SCHED();
                s0 = mfma32(kx[2], qf[2], s0); s1 = mfma32(kx[6], qf[2], s1); sm = mfma32(km[1], qf[1], sm); SCHED();
                s0 = mfma32(kx[3], qf[3], s0); s1 = mfma32(kx[7], qf[3], s1); sm = mfma32(km[2], qf[2], sm); sm = mfma32(km[3], qf[3], sm); SCHED();
                LBAR();
#pragma unroll
                for (int k = 0; k < 4; ++k) vm[k] = *(LAS u32x4*)(L + L_VM + k * 1024 + l16b);
                SCHED();
                const float nm = -rowmax(s0, s1, sm) * SCL2;
#pragma unroll
                for (int i = 0; i < 4; ++i) { s0[i] = __builtin_amdgcn_exp2f(fmaf(s0[i], SCL2, nm)); s1[i] = __builtin_amdgcn_exp2f(fmaf(s1[i], SCL2, nm)); }
                const u32x4 px = {pk(s0[0], s0[1]), pk(s0[2], s0[3]), pk(s1[0], s1[1]), pk(s1[2], s1[3])};
                const u32x4 pxl = {px.x & mlo, px.y & mlo, px.z & mlo, px.w & mlo};
                SCHED();
                f32x4 o[4];
                const f32x4 lx = mfma32(halves, px, z4);
#pragma unroll
                for (int q = 0; q < 4; ++q) o[q] = mfma32(vx[2 * q], pxl, z4);
                const u32x4 pxh = {px.x & ~mlo, px.y & ~mlo, px.z & ~mlo, px.w & ~mlo};
#pragma unroll
                for (int i = 0; i < 4; ++i) sm[i] = __builtin_amdgcn_exp2f(fmaf(sm[i], SCL2, nm));
                const u32 pm0 = pk(sm[0], sm[1]), pm1 = pk(sm[2], sm[3]);
                const u32x4 pmm = {pm0 & mlo, pm1 & mlo, pm0 & ~mlo, pm1 & ~mlo};
                SCHED();
                const f32x4 lm = mfma32(halves, pmm, z4);
                o[0] = mfma32(vx[1], pxh, o[0]); o[1] = mfma32(vx[3], pxh, o[1]);
                o[0] = mfma32(vm[0], pmm, o[0]); o[1] = mfma32(vm[1], pmm, o[1]);
                o[2] = mfma32(vx[5], pxh, o[2]); o[3] = mfma32(vx[7], pxh, o[3]);
                o[2] = mfma32(vm[2], pmm, o[2]); o[3] = mfma32(vm[3], pmm, o[3]);
                const float inv = __builtin_amdgcn_rcpf(fmaf(lx[0], 2.f, lm[0]));
                u32x4 af[2];
#pragma unroll
                for (int ks = 0; ks < 2; ++ks) { const f32x4 x = o[2 * ks] * inv, y = o[2 * ks + 1] * inv; af[ks] = u32x4{pk(x[0], x[1]), pk(x[2], x[3]), pk(y[0], y[1]), pk(y[2], y[3])}; }
#pragma unroll
                for (int ct = 0; ct < 4; ++ct) d[ct] = smf(af[0], wo[2 * ct], d[ct], idx);
#pragma unroll
                for (int ct = 0; ct < 4; ++ct) d[ct] = smf(af[1], wo[2 * ct + 1], d[ct], idx);
                r0 = u32x4{fb(hadd32(d[0][0], d[1][0])), fb(hadd32(d[0][1], d[1][1])), fb(hadd32(d[0][2], d[1][2])), fb(hadd32(d[0][3], d[1][3]))};
                r1 = u32x4{fb(hadd32(d[2][0], d[3][0])), fb(hadd32(d[2][1], d[3][1])), fb(hadd32(d[2][2], d[3][2])), fb(hadd32(d[2][3], d[3][3]))};
            }
            stg128(ws, pubp, px_, r0); stg128(ws, pubp, px_ + 4 * 8192, r1);
            TS(7);
            if (w == 4) {
                if (s < NSEG - 1) {
                    late |= wait_sched(tref + a.d3, a.m3);
                    TS(3);
                    float t0, t1; unsigned long long tn;
                    for (u32 sp = 0;; ++sp) {
                        MEMFENCE();
                        u32x4 v[8];
#pragma unroll
                        for (int k = 0; k < 8; ++k) v[k] = ldg(ws, l16b + (k & 3) * 1024, px_ + cu * 8192 + (k >> 2) * 4096);
                        asm volatile("s_memrealtime %0" : "=s"(tn) : "v"(v[6].x) : "memory");
                        const f32x4 acc = ((__builtin_bit_cast(f32x4, v[0]) + __builtin_bit_cast(f32x4, v[1])) + (__builtin_bit_cast(f32x4, v[2]) + __builtin_bit_cast(f32x4, v[3])))
                            + ((__builtin_bit_cast(f32x4, v[4]) + __builtin_bit_cast(f32x4, v[5])) + (__builtin_bit_cast(f32x4, v[6]) + __builtin_bit_cast(f32x4, v[7])));
                        t0 = hadd32(acc[0], acc[2]); t1 = hadd32(acc[1], acc[3]);
                        if (__builtin_expect(!__any(__builtin_isunordered(t0, t1)), 1)) break;
                        u32 m = 0;
#pragma unroll
                        for (int k = 0; k < 8; ++k) m = max(m, umax4(v[k]));
                        if (!__any(m == SENT)) break;
                        FAIL(12); late = true;
                        if (sp > cap) { give_up(ws, 0x500 + s); cap = 0; break; }
                    }
                    { auto r = __builtin_amdgcn_permlane16_swap(fb(t0), fb(t0), false, false); t0 = bf(r[0]) + bf(r[1]); }
                    { auto r = __builtin_amdgcn_permlane16_swap(fb(t1), fb(t1), false, false); t1 = bf(r[0]) + bf(r[1]); }
                    u32 val = pk(t0, t1);
                    asm volatile("s_waitcnt lgkmcnt(0)" : "+s"(tn) :: "memory");
                    if (lane == 16) val = ((u32)tn & ~1u) | (late ? 1u : 0u);
                    val = min(val, SENT - 2);
                    if (late) FAIL(13);
                    if (pubm_on) stg32(ws, pubm, MX_OFF + ((s + 1) & 3) * MX_SLOT, val);
                }
            } else if (w == 5 && !(a.flags & 1)) {
                f32x4 sm = z4, s0 = z4, s1 = z4;
#pragma unroll
                for (int ks = 0; ks < 4; ++ks) { const u32x4 qq = *(LAS u32x4*)(xb + L_QX + ks * 1024); sm = mfma32(km[ks], qq, sm); s0 = mfma32(kx[ks], qq, s0); s1 = mfma32(kx[4 + ks], qq, s1); }
                const int rq = 16 * qt + l16;
                constexpr float MASKV = -1e9f / 0.08838834764831845f;
#pragma unroll
                for (int i = 0; i < 4; ++i) { if (4 * g + i > rq) s0[i] = MASKV; if (16 + 4 * g + i > rq) s1[i] = MASKV; }
                const float nm = -rowmax(s0, s1, sm) * SCL2;
#pragma unroll
                for (int i = 0; i < 4; ++i) { sm[i] = __builtin_amdgcn_exp2f(fmaf(sm[i], SCL2, nm)); s0[i] = __builtin_amdgcn_exp2f(fmaf(s0[i], SCL2, nm)); s1[i] = __builtin_amdgcn_exp2f(fmaf(s1[i], SCL2, nm)); }
                const u32 pm0 = pk(sm[0], sm[1]), pm1 = pk(sm[2], sm[3]);
                const u32x4 pe = {pm0, pm1, 0u, 0u}, po = {0u, 0u, pm0, pm1};
                const u32x4 px = {pk(s0[0], s0[1]), pk(s0[2], s0[3]), pk(s1[0], s1[1]), pk(s1[2], s1[3])};
                const f32x4 lx = mfma32(halves, px, z4), lm = mfma32(halves, pe, z4);
                f32x4 o[4];
#pragma unroll
                for (int t = 0; t < 4; ++t) {
                    const u32x4 vxt = eh ? vx[4 + t] : vx[t], vmt = eh ? vm[2 + (t >> 1)] : vm[t >> 1];
                    o[t] = mfma32(vxt, px, z4); o[t] = mfma32(vmt, (t & 1) ? po : pe, o[t]);
                }
                const float inv = __builtin_amdgcn_rcpf(fmaf(lx[0], 2.f, lm[0]));
#pragma unroll
                for (int p = 0; p < 2; ++p) {
                    const f32x4 x = o[2 * p] * inv, y = o[2 * p + 1] * inv;
                    stg128(ws, l16b + p * 1024, OAF_OFF + ((s * 2 + qt) * 32 + h) * 4096 + eh * 2048, u32x4{pk(x[0], x[1]), pk(x[2], x[3]), pk(y[0], y[1]), pk(y[2], y[3])});
                }
                DRAIN();
                if (lane == 0) count_up(a.ws, OAC_OFF + (h >> 1) * 256 + s * 4);
            }
            TS(8);
            LBAR();
            TS(9);
        }
    }
    TSOUT;
}

__device__ __forceinline__ void wo_role(const Args& a, LAS char* L, const rsrc_t ws, const int id) {
    const int tid = threadIdx.x, lane = tid & 63, w = __builtin_amdgcn_readfirstlane(tid >> 6);
    const int kh = id >> 4, nb = id & 15, g = lane >> 4, l16 = lane & 15;
    u32 cap = SPIN_CAP;
    u32x4 wr[8][4];
#pragma unroll
    for (int rnd = 0; rnd < 2; ++rnd) {
#pragma unroll
        for (int i = 0; i < 16; ++i) {
            const int piece = i * 8 + w;
            const u16* src = a.Wo + (size_t)(2048 * kh + 1024 * rnd + piece * 8 + (lane >> 3)) * 1024 + nb * 64 + (lane & 7) * 8;
            __builtin_amdgcn_global_load_lds((const u32*)src, (LAS u32*)(L + piece * 1024), 16, 0, 0);
        }
        DRAIN(); LBAR();
        if ((w >> 2) == rnd) {
            LAS char* base = L + ((w & 3) * 256 + 4 * g + (l16 >> 2)) * 128 + (l16 & 3) * 8;
#pragma unroll
            for (int ks = 0; ks < 8; ++ks)
#pragma unroll
                for (int ct = 0; ct < 4; ++ct) { const u32x2 x0 = trrd(base + ks * 4096 + ct * 32), x1 = trrd(base + ks * 4096 + 2048 + ct * 32); wr[ks][ct] = u32x4{x0.x, x0.y, x1.x, x1.y}; }
        }
        LBAR();
    }
    const f32x4 z4 = {0.f, 0.f, 0.f, 0.f};
    u32 l16b = lane * 16; PIN(l16b);
    f32x4 keep = z4;
    u16* const outp = a.out + (size_t)(16 * (w >> 2) + l16) * 2048 + nb * 64 + 16 * (w & 3) + 4 * g;
    auto finish = [&](int s) {
        poll_words(ws, WF_OFF + nb * 256 + s * 4, 1, lane, cap, 0x800 + s);
        const f32x4 p = __builtin_bit_cast(f32x4, ldg(ws, l16b, WP_OFF + (s * 16 + nb) * 8192 + w * 1024));
        const f32x4 r = keep + p;
        *(u32x2*)(outp + (size_t)s * 32 * 2048) = u32x2{pk(r[0], r[1]), pk(r[2], r[3])};
    };
#pragma unroll 1
    for (int s = 0; s < NSEG; ++s) {
        poll_count(ws, OAC_OFF + (8 * kh + w) * 256 + s * 4, 7u, cap, 0x700 + s);
        u32x4 x[2][8];
#pragma unroll
        for (int ks = 0; ks < 8; ++ks)
#pragma unroll
            for (int rt = 0; rt < 2; ++rt) x[rt][ks] = ldg(ws, l16b + (ks & 3) * 1024, OAF_OFF + ((s * 2 + rt) * 32 + 16 * kh + 2 * w + (ks >> 2)) * 4096);
        if (kh == 0 && s > 0) finish(s - 1);
        f32x4 acc[2][4];
#pragma unroll
        for (int rt = 0; rt < 2; ++rt)
#pragma unroll
            for (int ct = 0; ct < 4; ++ct) acc[rt][ct] = z4;
#pragma unroll
        for (int ks = 0; ks < 8; ++ks)
#pragma unroll
            for (int rt = 0; rt < 2; ++rt)
#pragma unroll
                for (int ct = 0; ct < 4; ++ct) acc[rt][ct] = mfma32(wr[ks][ct], x[rt][ks], acc[rt][ct]);
        LAS char* red = L + (s & 1) * 65536;
#pragma unroll
        for (int rt = 0; rt < 2; ++rt)
#pragma unroll
            for (int ct = 0; ct < 4; ++ct) *(LAS f32x4*)(red + w * 8192 + (rt * 4 + ct) * 1024 + l16b) = acc[rt][ct];
        LBAR();
        f32x4 t[8];
#pragma unroll
        for (int k = 0; k < 8; ++k) t[k] = *(LAS f32x4*)(red + k * 8192 + w * 1024 + l16b);
        keep = ((t[0] + t[1]) + (t[2] + t[3])) + ((t[4] + t[5]) + (t[6] + t[7]));
        if (kh == 1) {
            stg128(ws, l16b, WP_OFF + (s * 16 + nb) * 8192 + w * 1024, __builtin_bit_cast(u32x4, keep));
            DRAIN(); LBAR();
            if (tid == 0) stg32(ws, 0, WF_OFF + nb * 256 + s * 4, 0u);
        }
    }
    if (kh == 0) finish(NSEG - 1);
}

__device__ __forceinline__ u32x4 mk_srd(const void* p, u32 bytes) {
    const unsigned long long v = (unsigned long long)p;
    return u32x4{(u32)__builtin_amdgcn_readfirstlane((u32)v), (u32)__builtin_amdgcn_readfirstlane((u32)(v >> 32) & 0xffffu), bytes, 0x00020000u};
}
__device__ __forceinline__ void dma16(const u32x4 srd, u32 dst, u32 vo, u32 so) {
    asm volatile("s_mov_b32 m0, %0\n\ts_nop 0\n\tbuffer_load_dwordx4 %1, %2, %3 offen lds" :: "s"(dst), "v"(vo), "s"(srd), "s"(so) : "memory");
}
#define LGKM0() __builtin_amdgcn_s_waitcnt(0xC07F)
constexpr int G_BUF = 49152, G_B = 32768;
template <bool SWAP>
__device__ __forceinline__ void gemm_tile(LAS char* L, const u32x4 ra, const u32 aoff, const int lda, const u32x4 rw, const u32 woff, const int ldw, f32x4 (&acc)[4][4]) {
    const int tid = threadIdx.x, lane = tid & 63, w = __builtin_amdgcn_readfirstlane(tid >> 6);
    const int wm = w >> 1, wn = w & 1, g = lane >> 4, l16 = lane & 15;
    const u32 va = (tid >> 3) * lda * 2 + (((tid & 7) ^ ((tid >> 3) & 7)) << 4);
    const int rb = tid >> 4;
    const u32 vb = rb * ldw * 2 + (((tid & 15) ^ (((rb & 3) | (((rb >> 3) & 1) << 2)) << 1)) << 4);
    const u32 l0 = (u32)(size_t)L + w * 1024;
    auto stage = [&](int kt, int b) {
#pragma unroll
        for (int i = 0; i < 4; ++i) dma16(ra, l0 + b * G_BUF + i * 8192, va, aoff + kt * 128 + i * 128 * lda);
#pragma unroll
        for (int i = 0; i < 2; ++i) dma16(rw, l0 + b * G_BUF + G_B + i * 8192, vb, woff + (kt * 64 + i * 32) * ldw * 2);
    };
    LAS char* const ap0 = L + (64 * wm + l16) * 128 + ((g ^ (l16 & 7)) << 4);
    LAS char* const ap1 = L + (64 * wm + l16) * 128 + (((4 + g) ^ (l16 & 7)) << 4);
    const int q = l16 >> 2, f = q | ((g & 1) << 2);
    LAS char* bp[4];
#pragma unroll
    for (int n = 0; n < 4; ++n) bp[n] = L + G_B + (8 * g + q) * 256 + (((4 * wn + n) ^ f) << 5) + (l16 & 3) * 8;
    auto rdA = [&](u32x4 (&r)[4], int b, int kk) {
#pragma unroll
        for (int m = 0; m < 4; ++m) r[m] = *(LAS u32x4*)((kk ? ap1 : ap0) + b * G_BUF + m * 2048);
    };
    auto rdB = [&](u32x4 (&r)[4], int b, int kk) {
#pragma unroll
        for (int n = 0; n < 4; ++n) { const u32x2 x0 = trrd(bp[n] + b * G_BUF + kk * 8192), x1 = trrd(bp[n] + b * G_BUF + kk * 8192 + 1024); r[n] = u32x4{x0.x, x0.y, x1.x, x1.y}; }
    };
    auto mm = [&](const u32x4 (&A)[4], const u32x4 (&B)[4]) {
        __builtin_amdgcn_s_setprio(1);
#pragma unroll
        for (int m = 0; m < 4; ++m)
#pragma unroll
            for (int n = 0; n < 4; ++n) acc[m][n] = SWAP ? mfma32(B[n], A[m], acc[m][n]) : mfma32(A[m], B[n], acc[m][n]);
        __builtin_amdgcn_s_setprio(0);
    };
#pragma unroll
    for (int m = 0; m < 4; ++m)
#pragma unroll
        for (int n = 0; n < 4; ++n) acc[m][n] = f32x4{0.f, 0.f, 0.f, 0.f};
    constexpr int NT = 16;
    u32x4 A0[4], B0[4], A1[4], B1[4];
    stage(0, 0); stage(1, 1); stage(2, 2);
    asm volatile("s_waitcnt vmcnt(12)\n\ts_barrier" ::: "memory");
    rdA(A0, 0, 0); rdB(B0, 0, 0);
    auto step = [&](int kt, int b, int bn) {
        rdA(A1, b, 1); rdB(B1, b, 1);
        SCHED();
        mm(A0, B0);
        SCHED();
        if (kt + 2 < NT) asm volatile("s_waitcnt vmcnt(6) lgkmcnt(0)\n\ts_barrier" ::: "memory"); else asm volatile("s_waitcnt vmcnt(0) lgkmcnt(0)\n\ts_barrier" ::: "memory");
        if (kt + 3 < NT) stage(kt + 3, b);
        if (kt + 1 < NT) { rdA(A0, bn, 0); rdB(B0, bn, 0); }
        SCHED();
        mm(A1, B1);
        SCHED();
    };
#pragma unroll 1
    for (int kt = 0; kt < 12; kt += 3) {
        LGKM0(); rdA(A1, 0, 1); rdB(B1, 0, 1); SCHED(); mm(A0, B0); SCHED();
        asm volatile("s_waitcnt vmcnt(6) lgkmcnt(0)\n\ts_barrier" ::: "memory");
        stage(kt + 3, 0); rdA(A0, 1, 0); rdB(B0, 1, 0); SCHED(); mm(A1, B1); SCHED();
        rdA(A1, 1, 1); rdB(B1, 1, 1); SCHED(); mm(A0, B0); SCHED();
        asm volatile("s_waitcnt vmcnt(6) lgkmcnt(0)\n\ts_barrier" ::: "memory");
        stage(kt + 4, 1); rdA(A0, 2, 0); rdB(B0, 2, 0); SCHED(); mm(A1, B1); SCHED();
        rdA(A1, 2, 1); rdB(B1, 2, 1); SCHED(); mm(A0, B0); SCHED();
        asm volatile("s_waitcnt vmcnt(6) lgkmcnt(0)\n\ts_barrier" ::: "memory");
        stage(kt + 5, 2); rdA(A0, 0, 0); rdB(B0, 0, 0); SCHED(); mm(A1, B1); SCHED();
    }
    step(12, 0, 1); step(13, 1, 2); step(14, 2, 0); step(15, 0, 1);
}
__device__ __forceinline__ u32x4 pk8(const f32x4& x, const f32x4& y) { return u32x4{pk(x[0], x[1]), pk(x[2], x[3]), pk(y[0], y[1]), pk(y[2], y[3])}; }

template <int Z>
__device__ __forceinline__ void swa_tile(LAS char* kp, LAS char* vp, const u32x4 qf, const int nmask, const int lane, f32x4& o0, f32x4& o1) {
    const int g = lane >> 4, c = lane & 15;
    const f32x4 z4 = {0.f, 0.f, 0.f, 0.f};
    constexpr float SC = 0.17677669529663687f, SCL2 = SC * 1.4426950408889634f, MASKV = -1e9f / SC;
    f32x4 sc[17];
#pragma unroll
    for (int t = 0; t < 17; ++t) sc[t] = mfma32(*(LAS u32x4*)(kp + (Z + t) * 1024), qf, z4);
#pragma unroll
    for (int i = 0; i < 4; ++i) { if (4 * g + i <= c) sc[0][i] = MASKV; if (4 * g + i > c) sc[16][i] = MASKV; }
    if (nmask > 0) {
#pragma unroll
        for (int t = 0; t < 16; ++t) if (t < nmask) sc[t] = f32x4{MASKV, MASKV, MASKV, MASKV};
    }
    float m = mx3(sc[16][0], sc[16][1], mx3(sc[16][2], sc[16][3], sc[16][0]));
#pragma unroll
    for (int t = 0; t < 16; ++t) m = mx3(mx3(m, sc[t][0], sc[t][1]), sc[t][2], sc[t][3]);
    { auto r = __builtin_amdgcn_permlane32_swap(fb(m), fb(m), false, false); m = mx2v(bf(r[0]), bf(r[1])); }
    { auto r = __builtin_amdgcn_permlane16_swap(fb(m), fb(m), false, false); m = mx2v(bf(r[0]), bf(r[1])); }
    const float nm = -m * SCL2;
    u32x2 p[18];
    p[Z ? 0 : 17] = u32x2{0u, 0u};
#pragma unroll
    for (int t = 0; t < 17; ++t) {
        const float e0 = __builtin_amdgcn_exp2f(fmaf(sc[t][0], SCL2, nm)), e1 = __builtin_amdgcn_exp2f(fmaf(sc[t][1], SCL2, nm));
        const float e2 = __builtin_amdgcn_exp2f(fmaf(sc[t][2], SCL2, nm)), e3 = __builtin_amdgcn_exp2f(fmaf(sc[t][3], SCL2, nm));
        p[Z + t] = u32x2{pk(e0, e1), pk(e2, e3)};
    }
    const u32x4 ones = {0x3F803F80u, 0x3F803F80u, 0x3F803F80u, 0x3F803F80u};
    f32x4 ls = z4; o0 = z4; o1 = z4;
#pragma unroll
    for (int m2 = 0; m2 < 9; ++m2) {
        const u32x4 pb = {p[2 * m2].x, p[2 * m2].y, p[2 * m2 + 1].x, p[2 * m2 + 1].y};
        o0 = mfma32(*(LAS u32x4*)(vp + m2 * 2048), pb, o0);
        o1 = mfma32(*(LAS u32x4*)(vp + m2 * 2048 + 1024), pb, o1);
        ls = mfma32(ones, pb, ls);
    }
    const float inv = __builtin_amdgcn_rcpf(ls[0]);
    o0 *= inv; o1 *= inv;
}

__device__ __forceinline__ void acquire_agent() { __builtin_amdgcn_fence(__ATOMIC_ACQUIRE, "agent"); DRAIN(); }

__device__ __forceinline__ void gemm_role(const Args& a, LAS char* L, const rsrc_t ws, const int gi) {
    const int tid = threadIdx.x, lane = tid & 63, w = __builtin_amdgcn_readfirstlane(tid >> 6);
    const int wm = w >> 1, wn = w & 1;
    u32 cap = SPIN_CAP;
    const u32 l16b = lane * 16;
    const u32x4 wsr = mk_srd(a.ws, 0x10000000);
    f32x4 acc[4][4];
#pragma unroll 1
    for (int r = 0; r < 10; ++r) {
        const bool pa = r < 8;
        const int t = gi + 96 * (r - 8);
        const int blk = pa ? r : t / 24, ct = pa ? gi & 31 : t % 24, mat = pa ? gi >> 5 : ct >> 3;
        const bool isv = pa ? mat == 1 : mat == 2;
        const u16* Wp = pa ? (mat == 0 ? a.Wk : (mat == 1 ? a.Wv : a.Wq)) : a.wqkv;
        const int ldw = pa ? 4096 : 3072;
        const u32x4 ra = mk_srd(pa ? a.Ax : a.Bx, 2048 * 1024 * 2);
        const u32x4 rw = mk_srd(Wp, 1024 * ldw * 2);
        u32 flag;
        if (isv) {
            gemm_tile<false>(L, ra, blk * 256 * 2048, 1024, rw, ct * 256, ldw, acc);
            const u32 base = pa ? VXF_OFF + (blk * 8 * 32 + ct) * 8192 + wm * 524288 + wn * 4096 : VB_OFF + (ct & 7) * 524288 + blk * 16384 + wm * 4096 + wn * 262144;
            const u32 sp_ = pa ? 262144 : 2048, sn1 = pa ? 2048 : 131072;
#pragma unroll
            for (int p = 0; p < 2; ++p)
#pragma unroll
                for (int n = 0; n < 4; ++n) stg128(ws, l16b, base + p * sp_ + (n >> 1) * sn1 + (n & 1) * 1024, pk8(acc[2 * p][n], acc[2 * p + 1][n]));
        } else {
            gemm_tile<true>(L, ra, blk * 256 * 2048, 1024, rw, ct * 256, ldw, acc);
            const u32 base = pa ? (mat == 0 ? KXF_OFF : QXF_OFF) + (blk * 8 * 32 + ct) * 8192 + wm * 524288 + wn * 2048
                                : (mat == 0 ? QB_OFF : KB_OFF) + (ct & 7) * 524288 + blk * 16384 + wm * 4096 + wn * 262144;
            const u32 sm1 = pa ? 262144 : 2048, sm0 = pa ? 4096 : 1024, sn = pa ? 1024 : 131072;
#pragma unroll
            for (int m = 0; m < 4; ++m)
#pragma unroll
                for (int p = 0; p < 2; ++p) stg128(ws, l16b, base + (m >> 1) * sm1 + (m & 1) * sm0 + p * sn, pk8(acc[m][2 * p], acc[m][2 * p + 1]));
        }
        flag = pa ? XF_OFF + ct * 128 + (blk * 3 + mat) * 4 : BF_OFF + (ct & 7) * 128 + (blk * 3 + mat) * 4;
        DRAIN(); LBAR();
        if (tid == 0) stg32(ws, 0, flag, 0u);
    }
#pragma unroll 1
    for (int u = gi; u < 256; u += 96) {
        const int blk = u >> 5, h = u & 31;
        if (w == 0) {
            const int b0 = max(blk - 1, 0);
            poll_words(ws, BF_OFF + (h >> 2) * 128 + b0 * 12, (blk - b0 + 1) * 3, lane, cap, 0x900 + u);
            acquire_agent();
        }
        LBAR();
#pragma unroll
        for (int i = 0; i < 4; ++i) {
            const int pc = i * 8 + w, half = pc >> 4;
            const u32 rel = (u32)(max(blk - 1 + half, 0) * 16 + (pc & 15)) * 1024;
            dma16(wsr, (u32)(size_t)L + pc * 1024, l16b, KB_OFF + h * 131072 + rel);
            dma16(wsr, (u32)(size_t)L + 32768 + pc * 1024, l16b, VB_OFF + h * 131072 + rel);
        }
        const u32x4 q0 = __builtin_amdgcn_raw_buffer_load_b128(ws, l16b, QB_OFF + h * 131072 + (blk * 16 + 2 * w) * 1024, 0);
        const u32x4 q1 = __builtin_amdgcn_raw_buffer_load_b128(ws, l16b + 1024, QB_OFF + h * 131072 + (blk * 16 + 2 * w) * 1024, 0);
        DRAIN(); LBAR();
        LAS char* kp = L + w * 2048 + l16b, *vp = L + 32768 + w * 2048 + l16b;
        const u32 ob = OB_OFF + (blk * 256 + 32 * w) * 2048 + h * 64;
        const u32 ov = (lane & 15) * 2048 + (lane >> 4) * 8;
        f32x4 o0, o1;
        swa_tile<0>(kp, vp, q0, blk ? 0 : 16 - 2 * w, lane, o0, o1);
        __builtin_amdgcn_raw_buffer_store_b64(u32x2{pk(o0[0], o0[1]), pk(o0[2], o0[3])}, ws, ov, ob, SC1);
        __builtin_amdgcn_raw_buffer_store_b64(u32x2{pk(o1[0], o1[1]), pk(o1[2], o1[3])}, ws, ov + 32, ob, SC1);
        swa_tile<1>(kp, vp, q1, blk ? 0 : 15 - 2 * w, lane, o0, o1);
        __builtin_amdgcn_raw_buffer_store_b64(u32x2{pk(o0[0], o0[1]), pk(o0[2], o0[3])}, ws, ov, ob + 16 * 2048, SC1);
        __builtin_amdgcn_raw_buffer_store_b64(u32x2{pk(o1[0], o1[1]), pk(o1[2], o1[3])}, ws, ov + 32, ob + 16 * 2048, SC1);
        DRAIN(); LBAR();
        if (tid == 0) count_up(a.ws, OBC_OFF + blk * 128);
    }
    if (gi < 64) {
        const int blk = gi >> 3, nt = gi & 7;
        if (w == 0) { poll_count(ws, OBC_OFF + blk * 128, 31u, cap, 0xA00 + gi); acquire_agent(); }
        LBAR();
        const u32x4 rw = mk_srd(a.wo, 1024 * 1024 * 2);
        gemm_tile<true>(L, wsr, OB_OFF + blk * 256 * 2048, 1024, rw, nt * 256, 1024, acc);
        u16* op = a.out + (size_t)(blk * 256 + 64 * wm + (lane & 15)) * 2048 + 1024 + nt * 128 + 64 * wn + 4 * (lane >> 4);
#pragma unroll
        for (int m = 0; m < 4; ++m)
#pragma unroll
            for (int n = 0; n < 4; ++n) *(u32x2*)(op + m * 16 * 2048 + n * 16) = u32x2{pk(acc[m][n][0], acc[m][n][1]), pk(acc[m][n][2], acc[m][n][3])};
    }
}

__global__ void __launch_bounds__(512, 2) mega_kernel(Args a) {
    extern __shared__ __attribute__((aligned(16))) char smem_[];
    LAS char* L = (LAS char*)smem_;
    const rsrc_t ws = __builtin_amdgcn_make_buffer_rsrc(a.ws, 0, 0x10000000, 0x00020000);
    const int b = blockIdx.x;
    if (b < 128) chain_role(a, L, ws, b);
    else if (b < 160) wo_role(a, L, ws, b - 128);
    else gemm_role(a, L, ws, b - 160);
}

int g_flags = 0, g_slack = 25 | (15 << 8), g_pa = 32;
int g_m1 = 23, g_m2 = 18, g_m2v = 3, g_m3 = 31;
int g_d1 = 0, g_d2 = 0, g_d2v = 0, g_d3 = 0, g_chain_only = 0;
extern "C" void kernel_launch(void* const* d_in, const int* in_sizes, int n_in, void* d_out, int out_size, void* d_ws, size_t ws_size, hipStream_t stream) {
    char* ws = (char*)d_ws;
    (void)hipFuncSetAttribute((const void*)mega_kernel, hipFuncAttributeMaxDynamicSharedMemorySize, LDS_BYTES);
    (void)hipMemsetAsync(ws + CTRL_OFF, 0xFF, CTRL_BYTES, stream);
    Args a{(const u16*)d_in[0], (const u16*)d_in[1], (const u16*)d_in[2], (const u16*)d_in[3], (const u16*)d_in[4], (const u16*)d_in[5], (const u16*)d_in[7], (const u16*)d_in[8], (const u16*)d_in[9],
           (u16*)d_out, ws, g_d1, g_d2, g_d2v, g_d3, g_m1, g_m2, g_m2v, g_m3, g_flags | (g_chain_only ? 4 : 0), g_slack, g_pa, 0};
    mega_kernel<<<g_chain_only ? 128 : 256, 512, LDS_BYTES, stream>>>(a);
}
```

```cpp
#include <hip/hip_runtime.h>
#include <hip/hip_bf16.h>

#define LAS __attribute__((address_space(3)))
typedef unsigned u32;
typedef unsigned short u16;
typedef u32 u32x2 __attribute__((ext_vector_type(2)));
typedef u32 u32x4 __attribute__((ext_vector_type(4)));
typedef u32 u32x8 __attribute__((ext_vector_type(8)));
typedef float f32x4 __attribute__((ext_vector_type(4)));
typedef short s16x4 __attribute__((ext_vector_type(4)));
typedef __bf16 bf16x2 __attribute__((ext_vector_type(2)));
typedef __bf16 bf16x8 __attribute__((ext_vector_type(8)));
typedef __bf16 bf16x16 __attribute__((ext_vector_type(16)));
typedef __amdgpu_buffer_rsrc_t rsrc_t;

constexpr int NSEG = 64;
constexpr int WO_DIRECT = 60;
constexpr u32 SENT = 0xFFFFFFFFu;
constexpr int SC1 = 16;
constexpr u32 CTRL_OFF = 0, CTRL_BYTES = 0xC000;
constexpr u32 TMO_OFF = 0;
constexpr u32 TS0_OFF = 0x400;
constexpr u32 XF_OFF = 0x1000;
constexpr u32 OAC_OFF = 0x2000;
constexpr u32 WF_OFF = 0x3000;
constexpr u32 BF_OFF = 0x4000;
constexpr u32 OBC_OFF = 0x4800;
constexpr u32 RDY_OFF = 0x8000;
constexpr u32 MX_OFF = 0x10000, MX_SLOT = 0x5000, MX_TS = 16384;
constexpr u32 QX_OFF = 0x40000, QX_SLOT = 32 * 8192;
constexpr u32 PX_OFF = 0x200000, PX_SLOT = 128 * 8192;
constexpr u32 KXF_OFF = 0x1000000, VXF_OFF = KXF_OFF + 8192, QXF_OFF = KXF_OFF + 16384, OAF_OFF = KXF_OFF + 24576, XH = 0x200000, XS = 0x8000;
constexpr u32 QB_OFF = 0x5000000, KB_OFF = 0x5400000, VB_OFF = 0x5800000, OB_OFF = 0x5C00000;
constexpr u32 WP_OFF = 0x6000000;
constexpr u32 DBG_OFF = 0x7000000;
constexpr int L_M = 0, L_TS = 131072, L_QK = 16384, L_VM = L_QK + 4096, L_KX = L_VM + 4096, L_VX = L_KX + 8192, L_QX = L_VX + 8192, L_XB = 20480;
constexpr int L_STG = 0;
constexpr int LDS_BYTES = 147456;

__device__ __forceinline__ u32x4 ldg(rsrc_t r, u32 vo, u32 so) { return __builtin_amdgcn_raw_buffer_load_b128(r, vo, so, SC1); }
__device__ __forceinline__ u32x2 ldg64(rsrc_t r, u32 vo, u32 so) { return __builtin_amdgcn_raw_buffer_load_b64(r, vo, so, SC1); }
__device__ __forceinline__ u32 ldg32(rsrc_t r, u32 vo, u32 so) { return __builtin_amdgcn_raw_buffer_load_b32(r, vo, so, SC1); }
__device__ __forceinline__ void stg128(rsrc_t r, u32 vo, u32 so, u32x4 v) {
    asm volatile("" : "+v"(v));
    __builtin_amdgcn_raw_buffer_store_b128(v, r, vo, so, SC1);
    asm volatile("s_nop 1" :: "v"(v) : "memory");
}
__device__ __forceinline__ void stg32(rsrc_t r, u32 vo, u32 so, u32 v) { __builtin_amdgcn_raw_buffer_store_b32(v, r, vo, so, SC1); }
__device__ __forceinline__ u32x2 trrd(LAS char* p) { return __builtin_bit_cast(u32x2, __builtin_amdgcn_ds_read_tr16_b64_v4i16((LAS s16x4*)p)); }
__device__ __forceinline__ f32x4 mfma32(u32x4 a, u32x4 b, f32x4 c) { return __builtin_amdgcn_mfma_f32_16x16x32_bf16(__builtin_bit_cast(bf16x8, a), __builtin_bit_cast(bf16x8, b), c, 0, 0, 0); }
__device__ __forceinline__ f32x4 mfma16(u32x2 a, u32x2 b, f32x4 c) { return __builtin_amdgcn_mfma_f32_16x16x16bf16_1k(__builtin_bit_cast(s16x4, a), __builtin_bit_cast(s16x4, b), c, 0, 0, 0); }
__device__ __forceinline__ f32x4 smf(u32x4 a, u32x8 b, f32x4 c, int idx) { return __builtin_amdgcn_smfmac_f32_16x16x64_bf16(__builtin_bit_cast(bf16x8, a), __builtin_bit_cast(bf16x16, b), c, idx, 0, 0); }
__device__ __forceinline__ u32 pk(float lo, float hi) { bf16x2 v = {(__bf16)lo, (__bf16)hi}; return __builtin_bit_cast(u32, v); }
__device__ __forceinline__ u32 fb(float f) { return __builtin_bit_cast(u32, f); }
__device__ __forceinline__ float bf(u32 u) { return __builtin_bit_cast(float, u); }
__device__ __forceinline__ u32 umax4(u32x4 v) { return max(max(v.x, v.y), max(v.z, v.w)); }
#define LBAR() asm volatile("s_waitcnt lgkmcnt(0)\n\ts_barrier" ::: "memory")
#define MEMFENCE() asm volatile("" ::: "memory")
__device__ __forceinline__ void nap(int n) { for (int i = 0; i < n; ++i) __builtin_amdgcn_s_sleep(1); }
__device__ __forceinline__ float hadd32(float a, float b) { auto r = __builtin_amdgcn_permlane32_swap(fb(a), fb(b), false, false); return bf(r[0]) + bf(r[1]); }
constexpr u32 OOBV = 0x20000000u;
constexpr u32 SPIN_CAP = 1u << 15;

struct Args { const u16* Ax; const u16* mem0; const u16* Wq; const u16* Wk; const u16* Wv; const u16* Wo; const u16* Bx; const u16* wqkv; const u16* wo; u16* out; char* ws; int d1, d2, d2v, d3, m1, m2, m2v, m3, flags, slack, pa, per, wd1, wd2, d3l, stag; };

__device__ __forceinline__ void give_up(rsrc_t ws, u32 code) { stg32(ws, 0, TMO_OFF, code); }
__device__ __forceinline__ float mx3(float a, float b, float c) { return __builtin_fmaxf(__builtin_fmaxf(a, b), c); }
__device__ __forceinline__ float mx2v(float a, float b) { float r; asm("v_max_f32 %0, %1, %2" : "=v"(r) : "v"(a), "v"(b)); return r; }
#define SCHED() __builtin_amdgcn_sched_barrier(0)
#define PIN(x) asm volatile("" : "+v"(x))
__device__ __forceinline__ float rowmax(const f32x4& a, const f32x4& b, const f32x4& c) {
    float m = mx3(mx3(mx3(a[0], a[1], a[2]), a[3], b[0]), mx3(b[1], b[2], b[3]), mx3(mx3(c[0], c[1], c[2]), c[3], a[3]));
    { auto r = __builtin_amdgcn_permlane32_swap(fb(m), fb(m), false, false); m = mx2v(bf(r[0]), bf(r[1])); }
    { auto r = __builtin_amdgcn_permlane16_swap(fb(m), fb(m), false, false); m = mx2v(bf(r[0]), bf(r[1])); }
    return m;
}
__device__ __forceinline__ u32 now32() { return (u32)__builtin_amdgcn_s_memrealtime(); }
__device__ __forceinline__ void wait_until(u32 t) { while ((int)(now32() - t) < 0) {} }
__device__ __forceinline__ void wait_sched(u32 t, int m) { const int d = (int)(t - now32()); nap((max(d, m) * 22) >> 6); }
__device__ __forceinline__ int wave_max(int m) {
    m = max(m, __builtin_amdgcn_update_dpp(m, m, 0xB1, 0xf, 0xf, false));
    m = max(m, __builtin_amdgcn_update_dpp(m, m, 0x4E, 0xf, 0xf, false));
    m = max(m, __builtin_amdgcn_update_dpp(m, m, 0x141, 0xf, 0xf, false));
    m = max(m, __builtin_amdgcn_update_dpp(m, m, 0x140, 0xf, 0xf, false));
    { auto r = __builtin_amdgcn_permlane16_swap((u32)m, (u32)m, false, false); m = max((int)r[0], (int)r[1]); }
    { auto r = __builtin_amdgcn_permlane32_swap((u32)m, (u32)m, false, false); m = max((int)r[0], (int)r[1]); }
    return m;
}
__device__ __forceinline__ u32 later(u32 x, u32 y) { return (int)(x - y) > 0 ? x : y; }
__device__ __forceinline__ bool peek_anchor(LAS char* p, int s, u32& t) {
    MEMFENCE();
    const u32x2 v = *(volatile LAS u32x2*)p;
    t = __builtin_amdgcn_readfirstlane(v.x);
    return __builtin_amdgcn_readfirstlane(v.y) == (u32)s + 1;
}
__device__ __forceinline__ u32 get_anchor(LAS char* p, int s) {
    for (;;) {
        MEMFENCE();
        const u32x2 v = *(volatile LAS u32x2*)p;
        if (__builtin_amdgcn_readfirstlane(v.y) == (u32)s + 1) return __builtin_amdgcn_readfirstlane(v.x);
        __builtin_amdgcn_s_sleep(1);
    }
}
#define TS(k)
#define TSDECL
#define TSZERO
#define TSUM(n)
#define TSOUT
#define FAIL(k)

__device__ __forceinline__ void poll_words(rsrc_t ws, u32 off, int n, int lane, u32& cap, u32 code) {
    for (u32 sp = 0;; ++sp) {
        MEMFENCE();
        const u32 v = ldg32(ws, min(lane, n - 1) * 4, off);
        if (__all(v != SENT)) break;
        if (sp > cap) { give_up(ws, code); cap = 0; break; }
        __builtin_amdgcn_s_sleep(4);
    }
}
__device__ __forceinline__ void poll_count(rsrc_t ws, u32 off, u32 want, u32& cap, u32 code) {
    for (u32 sp = 0;; ++sp) {
        MEMFENCE();
        const u32 v = __builtin_amdgcn_readfirstlane(ldg32(ws, 0, off));
        if (v == want) break;
        if (sp > cap) { give_up(ws, code); cap = 0; break; }
        __builtin_amdgcn_s_sleep(4);
    }
}
__device__ __forceinline__ void count_up(char* wsp, u32 off) { __hip_atomic_fetch_add((__attribute__((address_space(1))) u32*)(wsp + off), 1u, __ATOMIC_RELAXED, __HIP_MEMORY_SCOPE_AGENT); }
#define PST(k)
#define DRAIN() asm volatile("s_waitcnt vmcnt(0)" ::: "memory")

__device__ __forceinline__ void chain_role(const Args& a, LAS char* L, const rsrc_t ws, const int cu) {
    const int tid = threadIdx.x, lane = tid & 63, w = __builtin_amdgcn_readfirstlane(tid >> 6);
    const int h = cu & 31, j = cu >> 5;
    const int g = lane >> 4, l16 = lane & 15;
    const u32x4 sent4 = {SENT, SENT, SENT, SENT};
    u32 cap = SPIN_CAP;
    TSDECL;
#pragma unroll
    for (int sl = 0; sl < 4; ++sl) stg128(ws, tid * 16, PX_OFF + sl * PX_SLOT + cu * 8192, sent4);
    { const int sl = tid >> 7, t = tid & 127; const u32 o = t < 32 ? j * 512 + t * 16 : (t < 64 ? 2048 + j * 512 + (t - 32) * 16 : 4096 + j * 1024 + (t - 64) * 16);
      stg128(ws, sl * QX_SLOT + o, QX_OFF + h * 8192, sent4); }
    if (tid < 32) stg128(ws, (tid >> 3) * MX_SLOT + (tid & 7) * 16, MX_OFF + cu * 128, sent4);
    const bool rep = j == 0;
    if (rep && tid < 4) stg32(ws, tid * MX_SLOT, MX_OFF + MX_TS + h * 128, SENT);
    if (tid < 8) *(LAS u32*)(L + L_TS + tid * 4) = 0u;
    auto stage1 = [&]() {
#pragma unroll
        for (int i = 0; i < 16; ++i) {
            const int piece = i * 8 + w;
            const int mat = piece >> 6, row = (piece & 63) * 16 + (lane >> 2);
            const u16* src = (mat ? a.Wk : a.Wq) + (size_t)row * 4096 + h * 128 + j * 32 + (lane & 3) * 8;
            __builtin_amdgcn_global_load_lds((const u32*)src, (LAS u32*)(L + piece * 1024), 16, 0, 0);
        }
        asm volatile("s_waitcnt vmcnt(0)" ::: "memory");
        LBAR();
        PST(0);
        if (tid == 0) stg32(ws, 0, RDY_OFF + cu * 128, 0u);
    };
    auto stage2 = [&]() {
        LBAR();
#pragma unroll
        for (int i = 0; i < 16; ++i) {
            const int piece = i * 8 + w;
            const u16* src = piece < 64 ? a.Wv + (size_t)(piece * 16 + (lane >> 2)) * 4096 + h * 128 + j * 32 + (lane & 3) * 8
                                        : a.Wo + (size_t)(h * 128 + (piece - 64) * 2 + (lane >> 5)) * 1024 + j * 256 + (lane & 31) * 8;
            __builtin_amdgcn_global_load_lds((const u32*)src, (LAS u32*)(L + piece * 1024), 16, 0, 0);
        }
        asm volatile("s_waitcnt vmcnt(0)" ::: "memory");
        LBAR();
    };
    const int qt = j & 1, eh = j >> 1;
    auto stage3 = [&]() {
        LBAR();
        PST(1);
        u32 c[8];
#pragma unroll
        for (int r = 0; r < 8; ++r) c[r] = *(const u32*)(a.mem0 + r * 1024 + tid * 2);
        u32x4 lo = {(c[0] & 0xffff) | (c[1] << 16), (c[2] & 0xffff) | (c[3] << 16), (c[4] & 0xffff) | (c[5] << 16), (c[6] & 0xffff) | (c[7] << 16)};
        u32x4 hi = {(c[0] >> 16) | (c[1] & 0xffff0000u), (c[2] >> 16) | (c[3] & 0xffff0000u), (c[4] >> 16) | (c[5] & 0xffff0000u), (c[6] >> 16) | (c[7] & 0xffff0000u)};
        *(LAS u32x4*)(L + L_M + tid * 32) = lo; *(LAS u32x4*)(L + L_M + tid * 32 + 16) = hi;
        if (!(a.flags & 4)) poll_words(ws, XF_OFF + h * 128, 3, lane, cap, 2);
        PST(2);
        const u32x4 kx = ldg(ws, tid * 16, KXF_OFF + h * XH), vx = ldg(ws, tid * 16, VXF_OFF + h * XH);
        *(LAS u32x4*)(L + L_KX + tid * 16) = kx; *(LAS u32x4*)(L + L_VX + tid * 16) = vx;
        if (w < 4) *(LAS u32x4*)(L + L_QX + tid * 16) = ldg(ws, tid * 16, QXF_OFF + h * XH + qt * 4096);
        const u32x4 kx1 = ldg(ws, tid * 16, KXF_OFF + h * XH + XS), vx1 = ldg(ws, tid * 16, VXF_OFF + h * XH + XS);
        *(LAS u32x4*)(L + L_XB + L_KX + tid * 16) = kx1; *(LAS u32x4*)(L + L_XB + L_VX + tid * 16) = vx1;
        if (w < 4) *(LAS u32x4*)(L + L_XB + L_QX + tid * 16) = ldg(ws, tid * 16, QXF_OFF + h * XH + XS + qt * 4096);
        LBAR();
        if (w == 7) {
            for (u32 sp = 0;; ++sp) {
                MEMFENCE();
                const u32 r0 = ldg32(ws, lane * 128, RDY_OFF), r1 = ldg32(ws, lane * 128, RDY_OFF + 8192);
                if (__all(r0 != SENT && r1 != SENT)) break;
                if (sp > cap) { give_up(ws, 1); cap = 0; break; }
                __builtin_amdgcn_s_sleep(8);
            }
        }
        LBAR();
        PST(3);
    };
    const int idx = (l16 & 8) ? 0xEEEE : 0x4444;
    const f32x4 z4 = {0.f, 0.f, 0.f, 0.f};
    constexpr float SCL2 = 0.08838834764831845f * 1.4426950408889634f;
    u32 tref = now32() - 100000u;
    u32 l16b = lane * 16;
    PIN(l16b);
    LAS char* const mwr = L + L_M + w * 2048 + l16b;
    auto hop1 = [&](int s) {
        const u32 off = MX_OFF + (s & 3) * MX_SLOT + w * 2048;
        wait_sched(tref + a.d1, a.m1);
        MEMFENCE();
        u32x4 v0 = ldg(ws, l16b, off), v1 = ldg(ws, l16b + 1024, off);
        for (u32 sp = 0;; ++sp) {
            *(LAS u32x4*)(mwr) = v0; *(LAS u32x4*)(mwr + 1024) = v1;
            const bool n0 = umax4(v0) == SENT, n1 = umax4(v1) == SENT;
            if (__builtin_expect(!__any(n0 || n1), 1)) break;
            FAIL(10);
            if (sp > cap) { give_up(ws, 0x100 + s); cap = 0; break; }
            MEMFENCE();
            if (n0) v0 = ldg(ws, l16b, off);
            if (n1) v1 = ldg(ws, l16b + 1024, off);
        }
    };
    auto rd = [&](LAS char* p) { const u32x2 x0 = trrd(p), x1 = trrd(p + 256); return u32x4{x0.x, x0.y, x1.x, x1.y}; };

    if (w < 4) {
        u32x8 wq[16], wv[8];
        const int t = w & 1, kh = w >> 1;
        stage1();
        {
            LAS char* base = L + (w >> 1) * 65536 + (16 * (g & 1) + 8 * (g >> 1) + (l16 >> 2)) * 64 + t * 32 + (l16 & 3) * 8;
            LAS char* bA = base + (1 - kh) * 32768, *bB = base + kh * 32768;
#pragma unroll
            for (int ks = 0; ks < 16; ++ks)
#pragma unroll
                for (int q = 0; q < 4; ++q) { const u32x2 v = trrd((ks < 8 ? bA : bB) + (64 * (ks & 7) + 32 * (q >> 1) + 4 * (q & 1)) * 64); wq[ks][2 * q] = v.x; wq[ks][2 * q + 1] = v.y; }
        }
        stage2();
        {
            LAS char* base = L + (512 * kh + 16 * (g & 1) + 8 * (g >> 1) + (l16 >> 2)) * 64 + t * 32 + (l16 & 3) * 8;
#pragma unroll
            for (int ks = 0; ks < 8; ++ks)
#pragma unroll
                for (int q = 0; q < 4; ++q) { const u32x2 v = trrd(base + (64 * ks + 32 * (q >> 1) + 4 * (q & 1)) * 64); wv[ks][2 * q] = v.x; wv[ks][2 * q + 1] = v.y; }
        }
        stage3();
        LAS char* const mrd = L + L_M + (32 * (g >> 1) + 8 * (g & 1) + 4 * (l16 >> 3) + 2 * ((l16 >> 1) & 1) + ((l16 >> 2) & 1)) * 16 + (l16 & 1) * 8;
        LAS char* const mA = mrd + (1 - kh) * 8192, * const mB = mrd + kh * 8192;
        const u32 pub = (w >> 1) * 2048 + (32 * j + 16 * t + l16) * 16 + (g & 1) * 8 + (lane >> 5) * 4;
        int xw = 2;
        const u32 pubv = 4096 + j * 1024 + ((2 * kh + (g & 1)) * 16 + l16) * 16 + t * 8 + (lane >> 5) * 4;
#pragma unroll 1
        for (int s = 0; s < NSEG; ++s) {
            const u32 qx = QX_OFF + (s & 3) * QX_SLOT + h * 8192;
            TSZERO; TS(0);
            if (s > 0) hop1(s);
            f32x4 c0 = z4, e0 = z4;
            PIN(c0); PIN(e0);
            TS(1);
            LBAR();
            TS(2);
            u32x4 af[8];
#pragma unroll
            for (int ks = 0; ks < 7; ++ks) af[ks] = rd(mA + ks * 1024);
            SCHED();
#pragma unroll
            for (int ks = 0; ks < 16; ++ks) {
                const int nx = ks + 7;
                c0 = smf(af[ks & 7], wq[ks], c0, idx);
                if (nx < 16) af[nx & 7] = nx < 8 ? rd(mA + nx * 1024) : rd(mB + (nx - 8) * 1024);
                SCHED();
            }
            TS(3);
            e0 = smf(af[0], wv[0], e0, idx); e0 = smf(af[1], wv[1], e0, idx);
            SCHED();
            stg32(ws, pub, qx, min(pk(hadd32(c0[0], c0[2]), hadd32(c0[1], c0[3])), SENT - 1));
            SCHED();
            TS(4);
#pragma unroll
            for (int ks = 2; ks < 8; ++ks) e0 = smf(af[ks], wv[ks], e0, idx);
            stg32(ws, pubv, qx, min(pk(hadd32(e0[0], e0[2]), hadd32(e0[1], e0[3])), SENT - 1));
            TS(5);
            wait_sched(tref + a.d2, a.m2);
            TS(6);
            u32x4 qk = ldg(ws, l16b, qx + w * 1024);
            nap(a.m2v);
            u32x4 vv = ldg(ws, l16b, qx + 4096 + w * 1024);
            SCHED();
            for (u32 sp = 0;; ++sp) {
                *(LAS u32x4*)(mwr + (L_QK - L_M) - w * 1024) = qk;
                if (__builtin_expect(!__any(umax4(qk) == SENT), 1)) break;
                FAIL(11);
                if (sp > cap) { give_up(ws, 0x200 + s); cap = 0; break; }
                MEMFENCE();
                qk = ldg(ws, l16b, qx + w * 1024);
            }
            TS(7);
            LBAR();
            TS(8);
            unsigned long long tb2 = 0;
            const bool stamper = rep && w == 0;
            if (stamper) asm volatile("s_memrealtime %0" : "=s"(tb2) :: "memory");
            for (u32 sp = 0;; ++sp) {
                *(LAS u32x4*)(mwr + (L_VM - L_M) - w * 1024) = vv;
                if (__builtin_expect(!__any(umax4(vv) == SENT), 1)) break;
                FAIL(12);
                if (sp > cap) { give_up(ws, 0x300 + s); cap = 0; break; }
                MEMFENCE();
                vv = ldg(ws, l16b, qx + 4096 + w * 1024);
                if (stamper) asm volatile("s_memrealtime %0" : "=s"(tb2) :: "memory");
            }
            const u32 sx = MX_OFF + (s & 3) * MX_SLOT + MX_TS;
            if (stamper) {
                asm volatile("s_waitcnt lgkmcnt(0)" : "+s"(tb2) :: "memory");
                if (l16b == 0) stg32(ws, 0, sx + h * 128, min((u32)tb2, SENT - 1));
            }
            LBAR();
            u32x4 pf[5];
            {
                const int sn = min(s + 2, NSEG - 1);
                const u32 src = h * XH + sn * XS;
                pf[0] = ldg(ws, l16b, KXF_OFF + src + w * 2048); pf[1] = ldg(ws, l16b + 1024, KXF_OFF + src + w * 2048); pf[2] = ldg(ws, l16b, VXF_OFF + src + w * 2048); pf[3] = ldg(ws, l16b + 1024, VXF_OFF + src + w * 2048);
                pf[4] = ldg(ws, l16b, QXF_OFF + src + qt * 4096 + w * 1024);
            }
            SCHED();
            if (s > 0) {
                const int sl = (s - 1) & 3;
                u32x4 sn = sent4; PIN(sn);
                stg128(ws, l16b, PX_OFF + sl * PX_SLOT + cu * 8192 + w * 2048, sn);
                stg128(ws, l16b + 1024, PX_OFF + sl * PX_SLOT + cu * 8192 + w * 2048, sn);
                const u32 qb = QX_OFF + sl * QX_SLOT + h * 8192;
                if (w < 2) { if (l16b < 512) stg128(ws, l16b, qb + w * 2048 + j * 512, sn); }
                else if (w == 2) stg128(ws, l16b, qb + 4096 + j * 1024, sn);
                else { if (l16b < 128) stg128(ws, l16b, MX_OFF + sl * MX_SLOT + cu * 128, sn); if (rep && l16b == 0 && s > 1) stg32(ws, 0, MX_OFF + ((s - 2) & 3) * MX_SLOT + MX_TS + h * 128, sn.x); }
            }
            if (w == 1) {
                wait_sched(tref + a.d2v, a.pa);
                u32 t = SENT;
                for (u32 sp = 0;; ++sp) {
                    MEMFENCE();
                    if (t == SENT) t = ldg32(ws, (l16b & 496) * 8, sx);
                    if (__builtin_expect(!__any(t == SENT), 1)) break;
                    if (sp > 4096) break;
                }
                u32 an = tref + (u32)__builtin_amdgcn_readfirstlane(wave_max((int)(t - tref))) + a.d3;
                if ((int)(an - (tref + a.per)) > 0) { an += a.d3l; FAIL(13); } else an = tref + a.per;
                if (l16b == 0) *(LAS u32x2*)(L + L_TS) = u32x2{an, (u32)s + 1};
            }
            {
                LAS char* dst = mwr - L_M + xw * L_XB;
                *(LAS u32x4*)(dst + L_KX) = pf[0]; *(LAS u32x4*)(dst + L_KX + 1024) = pf[1]; *(LAS u32x4*)(dst + L_VX) = pf[2]; *(LAS u32x4*)(dst + L_VX + 1024) = pf[3];
                *(LAS u32x4*)(dst + L_QX - w * 1024) = pf[4];
                xw = xw == 2 ? 0 : xw + 1;
                if (((s + 3) & 7) == 0 && s + 3 < NSEG && !(a.flags & 4)) poll_words(ws, XF_OFF + h * 128 + ((s + 3) >> 3) * 12, 3, lane, cap, 0x600 + s);
            }
            tref = get_anchor(L + L_TS, s);
            TS(9); TSUM(10);
        }
    } else {
        const int ww = w - 4;
        u32x8 wo[8];
        stage1();
        stage2();
        {
            const int jj = l16 >> 2;
            LAS char* b2 = L + 65536 + (16 * (jj >> 1) + 32 * (g & 1) + 4 * (g >> 1) + (jj & 1)) * 512 + ww * 128 + (l16 & 3) * 8;
#pragma unroll
            for (int ct = 0; ct < 4; ++ct)
#pragma unroll
                for (int ks = 0; ks < 2; ++ks)
#pragma unroll
                    for (int m = 0; m < 4; ++m) { const u32x2 v = trrd(b2 + (64 * ks + 8 * (m >> 1) + 2 * (m & 1)) * 512 + ct * 32); wo[ct * 2 + ks][2 * m] = v.x; wo[ct * 2 + ks][2 * m + 1] = v.y; }
        }
        stage3();
        __builtin_amdgcn_s_setprio(2);
        LAS char* const qrd = L + L_QK + (4 * g + (l16 >> 2)) * 16 + (l16 & 1) * 8;
        const u32 pubp = (32 * j + 8 * ww + 2 * (lane >> 5) + (l16 >> 3)) * 8192 + h * 256 + ((l16 & 7) * 2 + (g & 1)) * 16;
        const u32 pubm = cu * 128 + l16 * 8 + (lane >> 5) * 4;
        u32 mlo = (l16 & 8) ? 0u : ~0u;
        u32x4 halves = {0x3F003F00u, 0x3F003F00u, 0x3F003F00u, 0x3F003F00u};
        PIN(halves); PIN(mlo);
        int xr = 0;
#pragma unroll 1
        for (int s = 0; s < NSEG; ++s) {
            const u32 qx = QX_OFF + (s & 3) * QX_SLOT + h * 8192;
            const u32 px_ = PX_OFF + (s & 3) * PX_SLOT;
            LAS char* const xb = L + xr * L_XB + l16b;
            xr = xr == 2 ? 0 : xr + 1;
            u32x4 kx[8], vx[8];
            TSZERO; TS(0);
#pragma unroll
            for (int k = 0; k < 8; ++k) kx[k] = *(LAS u32x4*)(xb + L_KX + k * 1024);
#pragma unroll
            for (int k = 0; k < 8; ++k) vx[k] = *(LAS u32x4*)(xb + L_VX + k * 1024);
            if (s > 0) hop1(s);
            LBAR();
            if (w == 6 && s > 0 && s - 1 < WO_DIRECT && lane == 0) count_up(a.ws, OAC_OFF + (h >> 1) * 256 + (s - 1) * 4);
            f32x4 d[4] = {z4, z4, z4, z4};
            PIN(d[0]); PIN(d[1]); PIN(d[2]); PIN(d[3]);
            TS(1);
            LBAR();
            TS(2);
            u32x4 qf[4], km[4];
            qf[0] = rd(qrd); qf[1] = rd(qrd + 512); km[0] = rd(qrd + 2048); qf[2] = rd(qrd + 1024); km[1] = rd(qrd + 2048 + 512); qf[3] = rd(qrd + 1536); km[2] = rd(qrd + 2048 + 1024);
            SCHED();
            km[3] = rd(qrd + 2048 + 1536);
            u32x4 r0, r1, vm[4];
            {
                f32x4 sm, s0, s1;
                s0 = mfma32(kx[0], qf[0], z4); s1 = mfma32(kx[4], qf[0], z4); SCHED();
                s0 = mfma32(kx[1], qf[1], s0); s1 = mfma32(kx[5], qf[1], s1); sm = mfma32(km[0], qf[0], z4); SCHED();
                s0 = mfma32(kx[2], qf[2], s0); s1 = mfma32(kx[6], qf[2], s1); sm = mfma32(km[1], qf[1], sm); SCHED();
                s0 = mfma32(kx[3], qf[3], s0); s1 = mfma32(kx[7], qf[3], s1); sm = mfma32(km[2], qf[2], sm); sm = mfma32(km[3], qf[3], sm); SCHED();
                LBAR();
#pragma unroll
                for (int k = 0; k < 4; ++k) vm[k] = *(LAS u32x4*)(L + L_VM + k * 1024 + l16b);
                SCHED();
                const float nm = -rowmax(s0, s1, sm) * SCL2;
#pragma unroll
                for (int i = 0; i < 4; ++i) { s0[i] = __builtin_amdgcn_exp2f(fmaf(s0[i], SCL2, nm)); s1[i] = __builtin_amdgcn_exp2f(fmaf(s1[i], SCL2, nm)); }
                const u32x4 px = {pk(s0[0], s0[1]), pk(s0[2], s0[3]), pk(s1[0], s1[1]), pk(s1[2], s1[3])};
                const u32x4 pxl = {px.x & mlo, px.y & mlo, px.z & mlo, px.w & mlo};
                SCHED();
                f32x4 o[4];
                const f32x4 lx = mfma32(halves, px, z4);
#pragma unroll
                for (int q = 0; q < 4; ++q) o[q] = mfma32(vx[2 * q], pxl, z4);
                const u32x4 pxh = {px.x & ~mlo, px.y & ~mlo, px.z & ~mlo, px.w & ~mlo};
#pragma unroll
                for (int i = 0; i < 4; ++i) sm[i] = __builtin_amdgcn_exp2f(fmaf(sm[i], SCL2, nm));
                const u32 pm0 = pk(sm[0], sm[1]), pm1 = pk(sm[2], sm[3]);
                const u32x4 pmm = {pm0 & mlo, pm1 & mlo, pm0 & ~mlo, pm1 & ~mlo};
                SCHED();
                const f32x4 lm = mfma32(halves, pmm, z4);
                o[0] = mfma32(vx[1], pxh, o[0]); o[1] = mfma32(vx[3], pxh, o[1]);
                o[0] = mfma32(vm[0], pmm, o[0]); o[1] = mfma32(vm[1], pmm, o[1]);
                o[2] = mfma32(vx[5], pxh, o[2]); o[3] = mfma32(vx[7], pxh, o[3]);
                o[2] = mfma32(vm[2], pmm, o[2]); o[3] = mfma32(vm[3], pmm, o[3]);
                const float inv = __builtin_amdgcn_rcpf(fmaf(lx[0], 2.f, lm[0]));
                u32x4 af[2];
#pragma unroll
                for (int ks = 0; ks < 2; ++ks) { const f32x4 x = o[2 * ks] * inv, y = o[2 * ks + 1] * inv; af[ks] = u32x4{pk(x[0], x[1]), pk(x[2], x[3]), pk(y[0], y[1]), pk(y[2], y[3])}; }
#pragma unroll
                for (int ct = 0; ct < 4; ++ct) d[ct] = smf(af[0], wo[2 * ct], d[ct], idx);
#pragma unroll
                for (int ct = 0; ct < 4; ++ct) d[ct] = smf(af[1], wo[2 * ct + 1], d[ct], idx);
                r0 = u32x4{fb(hadd32(d[0][0], d[1][0])), fb(hadd32(d[0][1], d[1][1])), fb(hadd32(d[0][2], d[1][2])), fb(hadd32(d[0][3], d[1][3]))};
                r1 = u32x4{fb(hadd32(d[2][0], d[3][0])), fb(hadd32(d[2][1], d[3][1])), fb(hadd32(d[2][2], d[3][2])), fb(hadd32(d[2][3], d[3][3]))};
            }
            stg128(ws, pubp, px_, r0); stg128(ws, pubp, px_ + 4 * 8192, r1);
            TS(7);
            if (w == 4) {
                if (s < NSEG - 1) {
                    {
                        u32 t3;
                        if (!peek_anchor(L + L_TS, s, t3)) {
                            const int d = (int)(tref + a.per - now32());
                            if (d > a.m3) { nap((d * 22) >> 6); if (peek_anchor(L + L_TS, s, t3) && t3 != tref + a.per) wait_sched(t3, 0); }
                            else wait_sched(get_anchor(L + L_TS, s), 0);
                        } else wait_sched(t3, 0);
                    }
                    TS(3);
                    u32x4 v[8];
                    MEMFENCE();
#pragma unroll
                    for (int k = 0; k < 8; ++k) v[k] = ldg(ws, l16b + (k & 3) * 1024, px_ + cu * 8192 + (k >> 2) * 4096);
                    MEMFENCE();
                    MEMFENCE();
                    auto sum8 = [&](float& t0, float& t1) {
                        const f32x4 acc = ((__builtin_bit_cast(f32x4, v[0]) + __builtin_bit_cast(f32x4, v[1])) + (__builtin_bit_cast(f32x4, v[2]) + __builtin_bit_cast(f32x4, v[3])))
                            + ((__builtin_bit_cast(f32x4, v[4]) + __builtin_bit_cast(f32x4, v[5])) + (__builtin_bit_cast(f32x4, v[6]) + __builtin_bit_cast(f32x4, v[7])));
                        t0 = hadd32(acc[0], acc[2]); t1 = hadd32(acc[1], acc[3]);
                    };
                    float t0, t1;
                    sum8(t0, t1);
                    if (__builtin_expect(__any(__builtin_isunordered(t0, t1)), 0)) {
                        for (u32 sp = 0;; ++sp) {
                            bool bad = false;
                            MEMFENCE();
#pragma unroll
                            for (int k = 0; k < 8; ++k) if (umax4(v[k]) == SENT) { bad = true; v[k] = ldg(ws, l16b + (k & 3) * 1024, px_ + cu * 8192 + (k >> 2) * 4096); }
                            if (!__any(bad)) break;
                            FAIL(12);
                            if (sp > cap) { give_up(ws, 0x500 + s); cap = 0; break; }
                        }
                        sum8(t0, t1);
                    }
                    { auto r = __builtin_amdgcn_permlane16_swap(fb(t0), fb(t0), false, false); t0 = bf(r[0]) + bf(r[1]); }
                    { auto r = __builtin_amdgcn_permlane16_swap(fb(t1), fb(t1), false, false); t1 = bf(r[0]) + bf(r[1]); }
                    const u32 val = min(pk(t0, t1), SENT - 1);
                    if (!(lane & 16)) stg32(ws, pubm, MX_OFF + ((s + 1) & 3) * MX_SLOT, val);
                }
            } else if (w == 5 && !(a.flags & 1)) {
                f32x4 sm = z4, s0 = z4, s1 = z4;
#pragma unroll
                for (int ks = 0; ks < 4; ++ks) { const u32x4 qq = *(LAS u32x4*)(xb + L_QX + ks * 1024); sm = mfma32(km[ks], qq, sm); s0 = mfma32(kx[ks], qq, s0); s1 = mfma32(kx[4 + ks], qq, s1); }
                const int rq = 16 * qt + l16;
                constexpr float MASKV = -1e9f / 0.08838834764831845f;
#pragma unroll
                for (int i = 0; i < 4; ++i) { if (4 * g + i > rq) s0[i] = MASKV; if (16 + 4 * g + i > rq) s1[i] = MASKV; }
                const float nm = -rowmax(s0, s1, sm) * SCL2;
#pragma unroll
                for (int i = 0; i < 4; ++i) { sm[i] = __builtin_amdgcn_exp2f(fmaf(sm[i], SCL2, nm)); s0[i] = __builtin_amdgcn_exp2f(fmaf(s0[i], SCL2, nm)); s1[i] = __builtin_amdgcn_exp2f(fmaf(s1[i], SCL2, nm)); }
                const u32 pm0 = pk(sm[0], sm[1]), pm1 = pk(sm[2], sm[3]);
                const u32x4 pe = {pm0, pm1, 0u, 0u}, po = {0u, 0u, pm0, pm1};
                const u32x4 px = {pk(s0[0], s0[1]), pk(s0[2], s0[3]), pk(s1[0], s1[1]), pk(s1[2], s1[3])};
                const f32x4 lx = mfma32(halves, px, z4), lm = mfma32(halves, pe, z4);
                f32x4 o[4];
#pragma unroll
                for (int t = 0; t < 4; ++t) {
                    const u32x4 vxt = eh ? vx[4 + t] : vx[t], vmt = eh ? vm[2 + (t >> 1)] : vm[t >> 1];
                    o[t] = mfma32(vxt, px, z4); o[t] = mfma32(vmt, (t & 1) ? po : pe, o[t]);
                }
                const float inv = __builtin_amdgcn_rcpf(fmaf(lx[0], 2.f, lm[0]));
#pragma unroll
                for (int p = 0; p < 2; ++p) {
                    const f32x4 x = o[2 * p] * inv, y = o[2 * p + 1] * inv;
                    stg128(ws, l16b + p * 1024, OAF_OFF + h * XH + s * XS + qt * 4096 + eh * 2048, u32x4{pk(x[0], x[1]), pk(x[2], x[3]), pk(y[0], y[1]), pk(y[2], y[3])});
                }
                DRAIN();
                if (s >= WO_DIRECT && lane == 0) count_up(a.ws, OAC_OFF + (h >> 1) * 256 + s * 4);
            }
            TS(8);
            TS(9);
            tref = get_anchor(L + L_TS, s);
        }
    }
    TSOUT;
}

__device__ __forceinline__ void wo_role(const Args& a, LAS char* L, const rsrc_t ws, const int id) {
    const int tid = threadIdx.x, lane = tid & 63, w = __builtin_amdgcn_readfirstlane(tid >> 6);
    const int kh = (id >> 2) & 1, nb = (id & 3) + 4 * (id >> 3), g = lane >> 4, l16 = lane & 15;
    u32 cap = SPIN_CAP;
    u32x4 wr[8][4];
    for (int i = 0; i < 3; ++i) __builtin_amdgcn_s_sleep(127);
#pragma unroll
    for (int rnd = 0; rnd < 2; ++rnd) {
#pragma unroll
        for (int i = 0; i < 16; ++i) {
            const int piece = i * 8 + w;
            const u16* src = a.Wo + (size_t)(2048 * kh + 1024 * rnd + piece * 8 + (lane >> 3)) * 1024 + nb * 64 + (lane & 7) * 8;
            __builtin_amdgcn_global_load_lds((const u32*)src, (LAS u32*)(L + piece * 1024), 16, 0, 0);
        }
        DRAIN(); LBAR();
        if ((w >> 2) == rnd) {
            LAS char* base = L + ((w & 3) * 256 + 4 * g + (l16 >> 2)) * 128 + (l16 & 3) * 8;
#pragma unroll
            for (int ks = 0; ks < 8; ++ks)
#pragma unroll
                for (int ct = 0; ct < 4; ++ct) { LAS char* p = base + ks * 4096 + ct * 32; const u32x2 x0 = trrd(p), x1 = trrd(p + 2048); wr[ks][ct] = u32x4{x0.x, x0.y, x1.x, x1.y}; }
        }
        LBAR();
    }
    const f32x4 z4 = {0.f, 0.f, 0.f, 0.f};
    u32 l16b = lane * 16; PIN(l16b);
    if (kh == 0) {
        const u32x4 sn = {SENT, SENT, SENT, SENT};
#pragma unroll 4
        for (int s = 0; s < NSEG; ++s) stg128(ws, l16b, WP_OFF + (s * 16 + nb) * 8192 + w * 1024, sn);
        DRAIN(); LBAR();
        if (tid == 0) stg32(ws, 0, WF_OFF + nb * 256, 0u);
    } else poll_words(ws, WF_OFF + nb * 256, 1, lane, cap, 0x800);
    f32x4 keep = z4;
    u32 tp = 0;
    u16* const outp = a.out + (size_t)(16 * (w >> 2) + l16) * 2048 + nb * 64 + 16 * (w & 3) + 4 * g;
    auto finish = [&](int s, u32x4 p) {
        for (u32 sp = 0; __any(umax4(p) == SENT); ++sp) {
            if (sp > cap) { give_up(ws, 0x801 + s); cap = 0; break; }
            MEMFENCE();
            p = ldg(ws, l16b, WP_OFF + (s * 16 + nb) * 8192 + w * 1024);
        }
        const f32x4 r = keep + __builtin_bit_cast(f32x4, p);
        *(u32x2*)(outp + (size_t)s * 32 * 2048) = u32x2{pk(r[0], r[1]), pk(r[2], r[3])};
    };
#pragma unroll 1
    for (int s = 0; s < NSEG; ++s) {
        if (w == 0) {
            if (s > 0 && s < WO_DIRECT) wait_sched(tp, 0);
            for (u32 sp = 0;; ++sp) {
                MEMFENCE();
                const u32 v = ldg32(ws, (lane & 7) * 256, OAC_OFF + 8 * kh * 256 + s * 4);
                const u32 t = ldg32(ws, 0, MX_OFF + (s & 3) * MX_SLOT + MX_TS);
                tp = __builtin_amdgcn_readfirstlane(t) + a.per + a.wd2;
                if (__all(v == 7u)) break;
                if (sp > cap) { give_up(ws, 0x700 + s); cap = 0; break; }
                if (s < NSEG - 1) __builtin_amdgcn_s_sleep(16); else __builtin_amdgcn_s_sleep(2);
            }
            if ((u32)(tp - now32()) > 1000u) tp = now32();
        }
        LBAR();
        u32x4 pp = {0u, 0u, 0u, 0u};
        if (kh == 0 && s > 0) pp = ldg(ws, l16b, WP_OFF + ((s - 1) * 16 + nb) * 8192 + w * 1024);
        u32x4 x[2][8];
        const int pace = s < WO_DIRECT ? a.wd1 : 0;
#pragma unroll
        for (int ks = 0; ks < 8; ++ks) {
#pragma unroll
            for (int rt = 0; rt < 2; ++rt) x[rt][ks] = ldg(ws, l16b + (ks & 3) * 1024, OAF_OFF + (16 * kh + 2 * w + (ks >> 2)) * XH + s * XS + rt * 4096);
            SCHED();
            if (ks < 7) nap(pace);
        }
        f32x4 acc[2][4];
#pragma unroll
        for (int rt = 0; rt < 2; ++rt)
#pragma unroll
            for (int ct = 0; ct < 4; ++ct) acc[rt][ct] = z4;
#pragma unroll
        for (int ks = 0; ks < 8; ++ks)
#pragma unroll
            for (int rt = 0; rt < 2; ++rt)
#pragma unroll
                for (int ct = 0; ct < 4; ++ct) acc[rt][ct] = mfma32(wr[ks][ct], x[rt][ks], acc[rt][ct]);
        LAS char* red = L + (s & 1) * 65536;
#pragma unroll
        for (int rt = 0; rt < 2; ++rt)
#pragma unroll
            for (int ct = 0; ct < 4; ++ct) *(LAS f32x4*)(red + w * 8192 + (rt * 4 + ct) * 1024 + l16b) = acc[rt][ct];
        if (kh == 0 && s > 0) finish(s - 1, pp);
        LBAR();
        f32x4 t[8];
#pragma unroll
        for (int k = 0; k < 8; ++k) t[k] = *(LAS f32x4*)(red + k * 8192 + w * 1024 + l16b);
        keep = ((t[0] + t[1]) + (t[2] + t[3])) + ((t[4] + t[5]) + (t[6] + t[7]));
        if (kh == 1) stg128(ws, l16b, WP_OFF + (s * 16 + nb) * 8192 + w * 1024, __builtin_bit_cast(u32x4, keep));
    }
    if (kh == 0) { nap(6); finish(NSEG - 1, u32x4{SENT, SENT, SENT, SENT}); }
}

__device__ __forceinline__ u32x4 mk_srd(const void* p, u32 bytes) {
    const unsigned long long v = (unsigned long long)p;
    return u32x4{(u32)__builtin_amdgcn_readfirstlane((u32)v), (u32)__builtin_amdgcn_readfirstlane((u32)(v >> 32) & 0xffffu), bytes, 0x00020000u};
}
__device__ __forceinline__ void dma16(const u32x4 srd, u32 dst, u32 vo, u32 so) {
    asm volatile("s_mov_b32 m0, %0\n\ts_nop 0\n\tbuffer_load_dwordx4 %1, %2, %3 offen lds" :: "s"(dst), "v"(vo), "s"(srd), "s"(so) : "memory");
}
#define LGKM0() __builtin_amdgcn_s_waitcnt(0xC07F)
constexpr int G_BUF = 49152, G_B = 32768;
struct Unit { u32x4 ra, rw; u32 aoff, woff, ldw2; };
__device__ __forceinline__ void g_dma(const Unit& u, u32 l0, u32 va, u32 vb, int q, int kt, int b) {
    if (q < 4) dma16(u.ra, l0 + b * G_BUF + q * 8192, va, u.aoff + kt * 128 + q * 64 * 2048);
    else dma16(u.rw, l0 + b * G_BUF + G_B + (q - 4) * 8192, vb, u.woff + (kt * 64 + (q - 4) * 32) * u.ldw2);
}
__device__ __forceinline__ u32 g_va(int tid) { return (tid >> 3) * 2048 + (((tid & 7) ^ ((tid >> 3) & 7)) << 4); }
__device__ __forceinline__ u32 g_vb(int tid, u32 ldw2) { const int rb = tid >> 4; return rb * ldw2 + (((tid & 15) ^ (((rb & 3) | (((rb >> 3) & 1) << 2)) << 1)) << 4); }
__device__ __forceinline__ void gemm_prime(LAS char* L, const Unit& u) {
    const int tid = threadIdx.x;
    const u32 l0 = (u32)(size_t)L + __builtin_amdgcn_readfirstlane(tid >> 6) * 1024, va = g_va(tid), vb = g_vb(tid, u.ldw2);
#pragma unroll
    for (int i = 0; i < 18; ++i) g_dma(u, l0, va, vb, i % 6, i / 6, i / 6);
}
template <bool SWAP>
__device__ __forceinline__ void gemm_tile(LAS char* L, const rsrc_t ws, const Unit& u, const u32 pflag, const bool has_nx, const Unit& nx, f32x4 (&acc)[4][4]) {
    const int tid = threadIdx.x, lane = tid & 63, w = __builtin_amdgcn_readfirstlane(tid >> 6);
    const int wm = w >> 1, wn = w & 1, g = lane >> 4, l16 = lane & 15;
    const u32 va = g_va(tid), vb = g_vb(tid, u.ldw2);
    const u32 l0 = (u32)(size_t)L + w * 1024;
    LAS char* const ap0 = L + (64 * wm + l16) * 128 + ((g ^ (l16 & 7)) << 4);
    LAS char* const ap1 = L + (64 * wm + l16) * 128 + (((4 + g) ^ (l16 & 7)) << 4);
    const int q = l16 >> 2, f = q | ((g & 1) << 2);
    LAS char* bp[4];
#pragma unroll
    for (int n = 0; n < 4; ++n) bp[n] = L + G_B + (8 * g + q) * 256 + (((4 * wn + n) ^ f) << 5) + (l16 & 3) * 8;
    auto rdA1 = [&](int m, int b, int kk) { return *(LAS u32x4*)((kk ? ap1 : ap0) + b * G_BUF + m * 2048); };
    auto rdB1 = [&](int n, int b, int kk) { const u32x2 x0 = trrd(bp[n] + b * G_BUF + kk * 8192), x1 = trrd(bp[n] + b * G_BUF + kk * 8192 + 1024); return u32x4{x0.x, x0.y, x1.x, x1.y}; };
    auto half = [&](const u32x4 (&A)[4], const u32x4 (&B)[4], u32x4 (&An)[4], u32x4 (&Bn)[4], bool rd, int b, int kk, auto&& gap) {
#pragma unroll
        for (int i = 0; i < 16; ++i) {
            const int m = i >> 2, n = i & 3;
            acc[m][n] = SWAP ? mfma32(B[n], A[m], acc[m][n]) : mfma32(A[m], B[n], acc[m][n]);
            if (rd && i < 8) { if (i & 1) Bn[i >> 1] = rdB1(i >> 1, b, kk); else An[i >> 1] = rdA1(i >> 1, b, kk); }
            gap(i);
            SCHED();
        }
    };
#pragma unroll
    for (int m = 0; m < 4; ++m)
#pragma unroll
        for (int n = 0; n < 4; ++n) acc[m][n] = f32x4{0.f, 0.f, 0.f, 0.f};
    u32x4 A0[4], B0[4], A1[4], B1[4];
    if (pflag) asm volatile("s_waitcnt vmcnt(20)\n\ts_barrier" ::: "memory"); else asm volatile("s_waitcnt vmcnt(12)\n\ts_barrier" ::: "memory");
#pragma unroll
    for (int i = 0; i < 4; ++i) { A0[i] = rdA1(i, 0, 0); B0[i] = rdB1(i, 0, 0); }
    auto step = [&](int kt, int b, int bn, int tail) {
        LGKM0();
        half(A0, B0, A1, B1, true, b, 1, [](int) {});
        if (tail >= 2) asm volatile("s_waitcnt vmcnt(0) lgkmcnt(0)\n\ts_barrier" ::: "memory");
        else if (b < 2 && tail == 0 && kt < 2 && pflag) asm volatile("s_waitcnt vmcnt(14) lgkmcnt(0)\n\ts_barrier" ::: "memory");
        else asm volatile("s_waitcnt vmcnt(6) lgkmcnt(0)\n\ts_barrier" ::: "memory");
        if (b == 2 && tail == 0 && kt == 2 && pflag && tid == 0) stg32(ws, 0, pflag, 0u);
        LGKM0();
        if (tail == 0) half(A1, B1, A0, B0, true, bn, 0, [&](int i) { if (i >= 4 && !(i & 1)) g_dma(u, l0, va, vb, (i - 4) >> 1, kt + 3, b); });
        else if (tail < 3) half(A1, B1, A0, B0, true, bn, 0, [](int) {});
        else if (has_nx) { const u32 vbn = g_vb(tid, nx.ldw2); half(A1, B1, A0, B0, false, 0, 0, [&](int i) { g_dma(nx, l0, va, vbn, i % 6, i / 6, i / 6); if (i >= 14) g_dma(nx, l0, va, vbn, i - 10, 2, 2); }); }
        else half(A1, B1, A0, B0, false, 0, 0, [](int) {});
    };
#pragma unroll 1
    for (int kt = 0; kt < 12; kt += 3) { step(kt, 0, 1, 0); step(kt + 1, 1, 2, 0); step(kt + 2, 2, 0, 0); }
    step(12, 0, 1, 0); step(13, 1, 2, 1); step(14, 2, 0, 2); step(15, 0, 1, 3);
}
__device__ __forceinline__ u32x4 pk8(const f32x4& x, const f32x4& y) { return u32x4{pk(x[0], x[1]), pk(x[2], x[3]), pk(y[0], y[1]), pk(y[2], y[3])}; }

template <int Z>
__device__ __forceinline__ void swa_tile(LAS char* kp, LAS char* vp, const u32x4 qf, const int nmask, const int lane, f32x4& o0, f32x4& o1) {
    const int g = lane >> 4, c = lane & 15;
    const f32x4 z4 = {0.f, 0.f, 0.f, 0.f};
    constexpr float SC = 0.17677669529663687f, SCL2 = SC * 1.4426950408889634f, MASKV = -1e9f / SC;
    f32x4 sc[17];
#pragma unroll
    for (int t = 0; t < 17; ++t) sc[t] = mfma32(*(LAS u32x4*)(kp + (Z + t) * 1024), qf, z4);
#pragma unroll
    for (int i = 0; i < 4; ++i) { if (4 * g + i <= c) sc[0][i] = MASKV; if (4 * g + i > c) sc[16][i] = MASKV; }
    if (nmask > 0) {
#pragma unroll
        for (int t = 0; t < 16; ++t) if (t < nmask) sc[t] = f32x4{MASKV, MASKV, MASKV, MASKV};
    }
    float m = mx3(sc[16][0], sc[16][1], mx3(sc[16][2], sc[16][3], sc[16][0]));
#pragma unroll
    for (int t = 0; t < 16; ++t) m = mx3(mx3(m, sc[t][0], sc[t][1]), sc[t][2], sc[t][3]);
    { auto r = __builtin_amdgcn_permlane32_swap(fb(m), fb(m), false, false); m = mx2v(bf(r[0]), bf(r[1])); }
    { auto r = __builtin_amdgcn_permlane16_swap(fb(m), fb(m), false, false); m = mx2v(bf(r[0]), bf(r[1])); }
    const float nm = -m * SCL2;
    u32x2 p[18];
    p[Z ? 0 : 17] = u32x2{0u, 0u};
#pragma unroll
    for (int t = 0; t < 17; ++t) {
        const float e0 = __builtin_amdgcn_exp2f(fmaf(sc[t][0], SCL2, nm)), e1 = __builtin_amdgcn_exp2f(fmaf(sc[t][1], SCL2, nm));
        const float e2 = __builtin_amdgcn_exp2f(fmaf(sc[t][2], SCL2, nm)), e3 = __builtin_amdgcn_exp2f(fmaf(sc[t][3], SCL2, nm));
        p[Z + t] = u32x2{pk(e0, e1), pk(e2, e3)};
    }
    const u32x4 ones = {0x3F803F80u, 0x3F803F80u, 0x3F803F80u, 0x3F803F80u};
    f32x4 ls = z4; o0 = z4; o1 = z4;
#pragma unroll
    for (int m2 = 0; m2 < 9; ++m2) {
        const u32x4 pb = {p[2 * m2].x, p[2 * m2].y, p[2 * m2 + 1].x, p[2 * m2 + 1].y};
        o0 = mfma32(*(LAS u32x4*)(vp + m2 * 2048), pb, o0);
        o1 = mfma32(*(LAS u32x4*)(vp + m2 * 2048 + 1024), pb, o1);
        ls = mfma32(ones, pb, ls);
    }
    const float inv = __builtin_amdgcn_rcpf(ls[0]);
    o0 *= inv; o1 *= inv;
}

__device__ __forceinline__ void acquire_agent() { __builtin_amdgcn_fence(__ATOMIC_ACQUIRE, "agent"); DRAIN(); }

__device__ __forceinline__ void gemm_role(const Args& a, LAS char* L, const rsrc_t ws, const int gi) {
    const int tid = threadIdx.x, lane = tid & 63, w = __builtin_amdgcn_readfirstlane(tid >> 6);
    const int wm = w >> 1, wn = w & 1;
    u32 cap = SPIN_CAP;
    const u32 l16b = lane * 16;
    const u32x4 wsr = mk_srd(a.ws, 0x10000000);
    f32x4 acc[4][4];
    auto unit = [&](int r) {
        const bool pa = r < 8;
        const int t = gi + 96 * (r - 8), blk = pa ? r : t / 24, ct = pa ? gi & 31 : t % 24, mat = gi >> 5;
        const u16* Wp = pa ? (mat == 0 ? a.Wk : (mat == 1 ? a.Wv : a.Wq)) : a.wqkv;
        const u32 ldw2 = (a.flags & 1024) ? 256 : (pa ? 8192 : 6144);
        if (a.flags & 1024) return Unit{mk_srd(pa ? a.Ax : a.Bx, 2048 * 1024 * 2), mk_srd(Wp, 0x600000), (u32)blk * 256 * 2048, (u32)(ct % 24) * 262144, ldw2};
        return Unit{mk_srd(pa ? a.Ax : a.Bx, 2048 * 1024 * 2), mk_srd(Wp, 1024 * ldw2), (u32)blk * 256 * 2048, (u32)ct * 256, ldw2};
    };
    Unit cur = unit(0);
    if (!(a.flags & 2048)) gemm_prime(L, cur); else {
        u32 junk = 0;
#pragma unroll
        for (int i = 0; i < 4; ++i) { const int ln = i * 512 + tid; asm volatile("buffer_load_dword %0, %1, %2, %3 offen" : "+v"(junk) : "v"((ln >> 1) * 8192 + (ln & 1) * 128), "s"(cur.rw), "s"(cur.woff) : "memory"); }
        if (tid < 342) asm volatile("buffer_load_dword %0, %1, %2, %3 offen" : "+v"(junk) : "v"(((gi >> 3) * 342 + tid) * 128), "s"(cur.ra), "s"(0) : "memory");
        gemm_prime(L, cur);
        asm volatile("s_waitcnt vmcnt(18)" : "+v"(junk) :: "memory");
    }
    u32 pflag = 0;
#pragma unroll 1
    for (int r = 0; r < 10; ++r) {
        const bool pa = r < 8;
        const int t = gi + 96 * (r - 8);
        const int blk = pa ? r : t / 24, ct = pa ? gi & 31 : t % 24, mat = pa ? gi >> 5 : ct >> 3;
        const bool isv = pa ? mat == 1 : mat == 2;
        const bool stream = r > 0 && r < 9;
        const Unit nx = unit(min(r + 1, 9));
        if (isv) {
            gemm_tile<false>(L, ws, cur, pflag, stream, nx, acc);
            const u32 base = pa ? VXF_OFF + ct * XH + (blk * 8 + wm * 2) * XS + wn * 4096 : VB_OFF + (ct & 7) * 524288 + blk * 16384 + wm * 4096 + wn * 262144;
            const u32 sp_ = pa ? XS : 2048, sn1 = pa ? 2048 : 131072;
#pragma unroll
            for (int p = 0; p < 2; ++p)
#pragma unroll
                for (int n = 0; n < 4; ++n) stg128(ws, (a.flags & 512) ? OOBV : l16b, base + p * sp_ + (n >> 1) * sn1 + (n & 1) * 1024, pk8(acc[2 * p][n], acc[2 * p + 1][n]));
        } else {
            gemm_tile<true>(L, ws, cur, pflag, stream, nx, acc);
            const u32 base = pa ? (mat == 0 ? KXF_OFF : QXF_OFF) + ct * XH + (blk * 8 + wm * 2) * XS + wn * 2048
                                : (mat == 0 ? QB_OFF : KB_OFF) + (ct & 7) * 524288 + blk * 16384 + wm * 4096 + wn * 262144;
            const u32 sm1 = pa ? XS : 2048, sm0 = pa ? 4096 : 1024, sn = pa ? 1024 : 131072;
#pragma unroll
            for (int m = 0; m < 4; ++m)
#pragma unroll
                for (int p = 0; p < 2; ++p) stg128(ws, (a.flags & 512) ? OOBV : l16b, base + (m >> 1) * sm1 + (m & 1) * sm0 + p * sn, pk8(acc[m][2 * p], acc[m][2 * p + 1]));
        }
        pflag = pa ? XF_OFF + ct * 128 + (blk * 3 + mat) * 4 : BF_OFF + (ct & 7) * 128 + (blk * 3 + mat) * 4;
        if (!stream) {
            DRAIN(); LBAR();
            if (tid == 0) stg32(ws, 0, pflag, 0u);
            pflag = 0;
            if (r == 0) gemm_prime(L, nx);
        }
        cur = nx;
        if (r == 1) for (int i = (gi >> 3) * a.stag; i > 0; --i) __builtin_amdgcn_s_sleep(16);
        PST(r);
    }
#pragma unroll 1
    for (int u = gi; u < 256; u += 96) {
        const int blk = u >> 5, h = u & 31;
        if (w == 0) {
            const int b0 = max(blk - 1, 0);
            poll_words(ws, BF_OFF + (h >> 2) * 128 + b0 * 12, (blk - b0 + 1) * 3, lane, cap, 0x900 + u);
            acquire_agent();
        }
        LBAR();
#pragma unroll
        for (int i = 0; i < 4; ++i) {
            const int pc = i * 8 + w, half = pc >> 4;
            const u32 rel = (u32)(max(blk - 1 + half, 0) * 16 + (pc & 15)) * 1024;
            dma16(wsr, (u32)(size_t)L + pc * 1024, l16b, KB_OFF + h * 131072 + rel);
            dma16(wsr, (u32)(size_t)L + 32768 + pc * 1024, l16b, VB_OFF + h * 131072 + rel);
        }
        const u32x4 q0 = __builtin_amdgcn_raw_buffer_load_b128(ws, l16b, QB_OFF + h * 131072 + (blk * 16 + 2 * w) * 1024, 0);
        const u32x4 q1 = __builtin_amdgcn_raw_buffer_load_b128(ws, l16b + 1024, QB_OFF + h * 131072 + (blk * 16 + 2 * w) * 1024, 0);
        DRAIN(); LBAR();
        LAS char* kp = L + w * 2048 + l16b, *vp = L + 32768 + w * 2048 + l16b;
        const u32 ob = OB_OFF + (blk * 256 + 32 * w) * 2048 + h * 64;
        const u32 ov = (lane & 15) * 2048 + (lane >> 4) * 8;
        f32x4 o0, o1;
        swa_tile<0>(kp, vp, q0, blk ? 0 : 16 - 2 * w, lane, o0, o1);
        __builtin_amdgcn_raw_buffer_store_b64(u32x2{pk(o0[0], o0[1]), pk(o0[2], o0[3])}, ws, ov, ob, SC1);
        __builtin_amdgcn_raw_buffer_store_b64(u32x2{pk(o1[0], o1[1]), pk(o1[2], o1[3])}, ws, ov + 32, ob, SC1);
        swa_tile<1>(kp, vp, q1, blk ? 0 : 15 - 2 * w, lane, o0, o1);
        __builtin_amdgcn_raw_buffer_store_b64(u32x2{pk(o0[0], o0[1]), pk(o0[2], o0[3])}, ws, ov, ob + 16 * 2048, SC1);
        __builtin_amdgcn_raw_buffer_store_b64(u32x2{pk(o1[0], o1[1]), pk(o1[2], o1[3])}, ws, ov + 32, ob + 16 * 2048, SC1);
        DRAIN(); LBAR();
        if (tid == 0) count_up(a.ws, OBC_OFF + blk * 128);
        PST(10 + u / 96);
    }
    if (gi < 64) {
        const int blk = gi >> 3, nt = gi & 7;
        if (w == 0) { poll_count(ws, OBC_OFF + blk * 128, 31u, cap, 0xA00 + gi); acquire_agent(); }
        LBAR();
        const Unit uo{wsr, mk_srd(a.wo, 1024 * 1024 * 2), OB_OFF + (u32)blk * 256 * 2048, (u32)nt * 256, 2048};
        gemm_prime(L, uo);
        gemm_tile<true>(L, ws, uo, 0u, false, uo, acc);
        u16* op = a.out + (size_t)(blk * 256 + 64 * wm + (lane & 15)) * 2048 + 1024 + nt * 128 + 64 * wn + 4 * (lane >> 4);
#pragma unroll
        for (int m = 0; m < 4; ++m)
#pragma unroll
            for (int n = 0; n < 4; ++n) *(u32x2*)(op + m * 16 * 2048 + n * 16) = u32x2{pk(acc[m][n][0], acc[m][n][1]), pk(acc[m][n][2], acc[m][n][3])};
    }
}

__global__ void __launch_bounds__(512, 2) mega_kernel(Args a) {
    extern __shared__ __attribute__((aligned(16))) char smem_[];
    LAS char* L = (LAS char*)smem_;
    const rsrc_t ws = __builtin_amdgcn_make_buffer_rsrc(a.ws, 0, 0x10000000, 0x00020000);
    const int b = blockIdx.x;
    int role;
    if (a.flags & 8) { const int x = b & 7, y = b >> 3; role = x < 4 ? (y >> 3) * 32 + (y & 7) * 4 + x : 128 + (x - 4) + 4 * y; }
    else role = b;
    if (role < 128) { if (!(a.flags & 256)) chain_role(a, L, ws, role); }
    else if (role < 160) { if (!(a.flags & (4 | 128))) wo_role(a, L, ws, role - 128); }
    else if (!(a.flags & 4)) gemm_role(a, L, ws, role - 160);
}

int g_stag = 1, g_wd1 = 0, g_wd2 = 480, g_d3l = 45, g_per = 340, g_flags = 0, g_slack = 2, g_pa = 12;
int g_m1 = 12, g_m2 = 15, g_m2v = 2, g_m3 = 6;
int g_d1 = 80, g_d2 = 184, g_d2v = 259, g_d3 = 80, g_chain_only = 0;
extern "C" void kernel_launch(void* const* d_in, const int* in_sizes, int n_in, void* d_out, int out_size, void* d_ws, size_t ws_size, hipStream_t stream) {
    char* ws = (char*)d_ws;
    (void)hipFuncSetAttribute((const void*)mega_kernel, hipFuncAttributeMaxDynamicSharedMemorySize, LDS_BYTES);
    (void)hipMemsetAsync(ws + CTRL_OFF, 0xFF, CTRL_BYTES, stream);
    Args a{(const u16*)d_in[0], (const u16*)d_in[1], (const u16*)d_in[2], (const u16*)d_in[3], (const u16*)d_in[4], (const u16*)d_in[5], (const u16*)d_in[7], (const u16*)d_in[8], (const u16*)d_in[9],
           (u16*)d_out, ws, g_d1, g_d2, g_d2v, g_d3, g_m1, g_m2, g_m2v, g_m3, g_flags | (g_chain_only ? 4 : 0), g_slack, g_pa, g_per, g_wd1, g_wd2, g_d3l, g_stag};
    mega_kernel<<<256, 512, LDS_BYTES, stream>>>(a);
}
```

```cpp
#include <hip/hip_runtime.h>
#include <hip/hip_bf16.h>

#define LAS __attribute__((address_space(3)))
typedef unsigned u32;
typedef unsigned short u16;
typedef u32 u32x2 __attribute__((ext_vector_type(2)));
typedef u32 u32x4 __attribute__((ext_vector_type(4)));
typedef u32 u32x8 __attribute__((ext_vector_type(8)));
typedef float f32x4 __attribute__((ext_vector_type(4)));
typedef short s16x4 __attribute__((ext_vector_type(4)));
typedef __bf16 bf16x2 __attribute__((ext_vector_type(2)));
typedef __bf16 bf16x8 __attribute__((ext_vector_type(8)));
typedef __bf16 bf16x16 __attribute__((ext_vector_type(16)));
typedef __amdgpu_buffer_rsrc_t rsrc_t;

constexpr int NSEG = 64;
constexpr int WO_DIRECT = 60;
constexpr u32 SENT = 0xFFFFFFFFu;
constexpr int SC1 = 16;
constexpr u32 CTRL_OFF = 0, CTRL_BYTES = 0xC000;
constexpr u32 TMO_OFF = 0;
constexpr u32 XF_OFF = 0x1000;
constexpr u32 OAC_OFF = 0x2000;
constexpr u32 WF_OFF = 0x3000;
constexpr u32 BF_OFF = 0x4000;
constexpr u32 OBC_OFF = 0x4800;
constexpr u32 RDY_OFF = 0x8000;
constexpr u32 NEAR_OFF = 0x400;
constexpr u32 MX_OFF = 0x200000, MX_SLOT = 0x40000;
constexpr u32 SX_OFF = 0x300000, SX_SLOT = 0x1000;
constexpr u32 QX_OFF = 0x400000, QX_SLOT = 0x80000;
constexpr u32 PX_OFF = 0x600000, PX_SLOT = 0x100000;
constexpr u32 KXF_OFF = 0x1000000, VXF_OFF = KXF_OFF + 8192, QXF_OFF = KXF_OFF + 16384, OAF_OFF = KXF_OFF + 24576, XH = 0x200000, XS = 0x8000;
constexpr u32 QB_OFF = 0x5000000, KB_OFF = 0x5400000, VB_OFF = 0x5800000, OB_OFF = 0x5C00000;
constexpr u32 WP_OFF = 0x6000000;
constexpr u32 WS_BYTES = 0x6800000;
constexpr u32 DBG_OFF = 0x7000000;
constexpr int L_M = 0, L_TS = 131072, L_QK = 16384, L_VM = L_QK + 4096, L_KX = L_VM + 4096, L_VX = L_KX + 8192, L_QX = L_VX + 8192, L_XB = 20480;
constexpr int L_STG = 0;
constexpr int LDS_BYTES = 147456;

__device__ __forceinline__ u32x4 ldg(rsrc_t r, u32 vo, u32 so) { return __builtin_amdgcn_raw_buffer_load_b128(r, vo, so, SC1); }
__device__ __forceinline__ u32x2 ldg64(rsrc_t r, u32 vo, u32 so) { return __builtin_amdgcn_raw_buffer_load_b64(r, vo, so, SC1); }
__device__ __forceinline__ u32 ldg32(rsrc_t r, u32 vo, u32 so) { return __builtin_amdgcn_raw_buffer_load_b32(r, vo, so, SC1); }
__device__ __forceinline__ void stg128(rsrc_t r, u32 vo, u32 so, u32x4 v) {
    asm volatile("" : "+v"(v));
    __builtin_amdgcn_raw_buffer_store_b128(v, r, vo, so, SC1);
    asm volatile("s_nop 1" :: "v"(v) : "memory");
}
__device__ __forceinline__ void stg32(rsrc_t r, u32 vo, u32 so, u32 v) { __builtin_amdgcn_raw_buffer_store_b32(v, r, vo, so, SC1); }
__device__ __forceinline__ u32x2 trrd(LAS char* p) { return __builtin_bit_cast(u32x2, __builtin_amdgcn_ds_read_tr16_b64_v4i16((LAS s16x4*)p)); }
__device__ __forceinline__ f32x4 mfma32(u32x4 a, u32x4 b, f32x4 c) { return __builtin_amdgcn_mfma_f32_16x16x32_bf16(__builtin_bit_cast(bf16x8, a), __builtin_bit_cast(bf16x8, b), c, 0, 0, 0); }
__device__ __forceinline__ f32x4 mfma16(u32x2 a, u32x2 b, f32x4 c) { return __builtin_amdgcn_mfma_f32_16x16x16bf16_1k(__builtin_bit_cast(s16x4, a), __builtin_bit_cast(s16x4, b), c, 0, 0, 0); }
__device__ __forceinline__ f32x4 smf(u32x4 a, u32x8 b, f32x4 c, int idx) { return __builtin_amdgcn_smfmac_f32_16x16x64_bf16(__builtin_bit_cast(bf16x8, a), __builtin_bit_cast(bf16x16, b), c, idx, 0, 0); }
__device__ __forceinline__ u32 pk(float lo, float hi) { bf16x2 v = {(__bf16)lo, (__bf16)hi}; return __builtin_bit_cast(u32, v); }
__device__ __forceinline__ u32x4 pk8(const f32x4& x, const f32x4& y) { return u32x4{pk(x[0], x[1]), pk(x[2], x[3]), pk(y[0], y[1]), pk(y[2], y[3])}; }
__device__ __forceinline__ u32 fb(float f) { return __builtin_bit_cast(u32, f); }
__device__ __forceinline__ float bf(u32 u) { return __builtin_bit_cast(float, u); }
__device__ __forceinline__ u32 umax4(u32x4 v) { return max(max(v.x, v.y), max(v.z, v.w)); }
#define LBAR() asm volatile("s_waitcnt lgkmcnt(0)\n\ts_barrier" ::: "memory")
#define MEMFENCE() asm volatile("" ::: "memory")
__device__ __forceinline__ void nap(int n) { for (int i = 0; i < n; ++i) __builtin_amdgcn_s_sleep(1); }
__device__ __forceinline__ float hadd32(float a, float b) { auto r = __builtin_amdgcn_permlane32_swap(fb(a), fb(b), false, false); return bf(r[0]) + bf(r[1]); }
constexpr u32 OOBV = 0x20000000u;
constexpr u32 SPIN_CAP = 1u << 15;

struct Args { const u16* Ax; const u16* mem0; const u16* Wq; const u16* Wk; const u16* Wv; const u16* Wo; const u16* Bx; const u16* wqkv; const u16* wo; u16* out; char* ws; int d1, d2, d2v, d3, m1, m2, m2v, m3, flags, slack, pa, per, wd1, wd2, d3l, stag, wsl, mq0, mup, mdn, mmax, e1, e2, e3; };

__device__ __forceinline__ void give_up(rsrc_t ws, u32 code) { stg32(ws, 0, TMO_OFF, code); }
__device__ __forceinline__ float mx3(float a, float b, float c) { return __builtin_fmaxf(__builtin_fmaxf(a, b), c); }
__device__ __forceinline__ float mx2v(float a, float b) { float r; asm("v_max_f32 %0, %1, %2" : "=v"(r) : "v"(a), "v"(b)); return r; }
#define SCHED() __builtin_amdgcn_sched_barrier(0)
#define PIN(x) asm volatile("" : "+v"(x))
__device__ __forceinline__ float rowmax(const f32x4& a, const f32x4& b, const f32x4& c) {
    float m = mx3(mx3(mx3(a[0], a[1], a[2]), a[3], b[0]), mx3(b[1], b[2], b[3]), mx3(mx3(c[0], c[1], c[2]), c[3], a[3]));
    { auto r = __builtin_amdgcn_permlane32_swap(fb(m), fb(m), false, false); m = mx2v(bf(r[0]), bf(r[1])); }
    { auto r = __builtin_amdgcn_permlane16_swap(fb(m), fb(m), false, false); m = mx2v(bf(r[0]), bf(r[1])); }
    return m;
}
__device__ __forceinline__ u32x4 widen(u32 v) {
    const auto r = __builtin_amdgcn_permlane32_swap(v, v, false, false);
    const auto lo = __builtin_amdgcn_permlane16_swap(r[0], r[0], false, false), hi = __builtin_amdgcn_permlane16_swap(r[1], r[1], false, false);
    return u32x4{lo[0], hi[0], lo[1], hi[1]};
}
__device__ __forceinline__ u32 now32() { return (u32)__builtin_amdgcn_s_memrealtime(); }
__device__ __forceinline__ void wait_until(u32 t) { while ((int)(now32() - t) < 0) {} }
__device__ __forceinline__ void sleep_from(u32 n, int d) {
    if (d > 30) { const u32 t = n + d; nap(((d - 20) * 19) >> 6); d = (int)(t - now32()); }
    nap((d * 22) >> 6);
}
__device__ __forceinline__ void wait_sched(u32 t, int m) { const u32 n = now32(); sleep_from(n, max((int)(t - n), m)); }
__device__ __forceinline__ int wave_max(int m) {
    m = max(m, __builtin_amdgcn_update_dpp(m, m, 0xB1, 0xf, 0xf, false));
    m = max(m, __builtin_amdgcn_update_dpp(m, m, 0x4E, 0xf, 0xf, false));
    m = max(m, __builtin_amdgcn_update_dpp(m, m, 0x141, 0xf, 0xf, false));
    m = max(m, __builtin_amdgcn_update_dpp(m, m, 0x140, 0xf, 0xf, false));
    { auto r = __builtin_amdgcn_permlane16_swap((u32)m, (u32)m, false, false); m = max((int)r[0], (int)r[1]); }
    { auto r = __builtin_amdgcn_permlane32_swap((u32)m, (u32)m, false, false); m = max((int)r[0], (int)r[1]); }
    return m;
}
__device__ __forceinline__ u32 later(u32 x, u32 y) { return (int)(x - y) > 0 ? x : y; }
__device__ __forceinline__ bool peek_anchor(LAS char* p, int s, u32& t) {
    MEMFENCE();
    const u32x2 v = *(volatile LAS u32x2*)p;
    t = __builtin_amdgcn_readfirstlane(v.x);
    return __builtin_amdgcn_readfirstlane(v.y) == (u32)s + 1;
}
__device__ __forceinline__ u32 get_anchor(LAS char* p, int s, int& mq) {
    for (;;) {
        MEMFENCE();
        const u32x4 v = *(volatile LAS u32x4*)p;
        if (__builtin_amdgcn_readfirstlane(v.w) == (u32)s + 1) { mq = __builtin_amdgcn_readfirstlane(v.z); return __builtin_amdgcn_readfirstlane(v.x); }
        __builtin_amdgcn_s_sleep(1);
    }
}
#define TS(k)
#define TSDECL
#define TSZERO
#define TSUM(n)
#define TSOUT
#define FAIL(k)

__device__ __forceinline__ void poll_words(rsrc_t ws, u32 off, int n, int lane, u32& cap, u32 code) {
    for (u32 sp = 0;; ++sp) {
        MEMFENCE();
        const u32 v = ldg32(ws, min(lane, n - 1) * 4, off);
        if (__all(v != SENT)) break;
        if (sp > cap) { give_up(ws, code); cap = 0; break; }
        __builtin_amdgcn_s_sleep(4);
    }
}
__device__ __forceinline__ void poll_count(rsrc_t ws, u32 off, u32 want, u32& cap, u32 code) {
    for (u32 sp = 0;; ++sp) {
        MEMFENCE();
        const u32 v = __builtin_amdgcn_readfirstlane(ldg32(ws, 0, off));
        if (v == want) break;
        if (sp > cap) { give_up(ws, code); cap = 0; break; }
        __builtin_amdgcn_s_sleep(4);
    }
}
__device__ __forceinline__ void count_up(char* wsp, u32 off) { __hip_atomic_fetch_add((__attribute__((address_space(1))) u32*)(wsp + off), 1u, __ATOMIC_RELAXED, __HIP_MEMORY_SCOPE_AGENT); }
#define PST(k)
#define DRAIN() asm volatile("s_waitcnt vmcnt(0)" ::: "memory")

__device__ __forceinline__ u32x4 mk_srd(const void* p, u32 bytes) {
    const unsigned long long v = (unsigned long long)p;
    return u32x4{(u32)__builtin_amdgcn_readfirstlane((u32)v), (u32)__builtin_amdgcn_readfirstlane((u32)(v >> 32) & 0xffffu), bytes, 0x00020000u};
}
__device__ __forceinline__ void dma16(const u32x4 srd, u32 dst, u32 vo, u32 so) {
    asm volatile("s_mov_b32 m0, %0\n\ts_nop 0\n\tbuffer_load_dwordx4 %1, %2, %3 offen lds" :: "s"(dst), "v"(vo), "s"(srd), "s"(so) : "memory");
}
__device__ __forceinline__ void chain_role(const Args& a, LAS char* L, const rsrc_t ws, const int cu) {
    const int tid = threadIdx.x, lane = tid & 63, w = __builtin_amdgcn_readfirstlane(tid >> 6);
    const int h = cu & 31, j = cu >> 5;
    const int g = lane >> 4, l16 = lane & 15;
    const u32x4 sent4 = {SENT, SENT, SENT, SENT};
    u32 cap = SPIN_CAP;
    TSDECL;
#pragma unroll
    for (int i = 0; i < 4; ++i) stg128(ws, (tid >> 8) * 8192 + (tid & 255) * 16, PX_OFF + i * PX_SLOT + (cu >> 1) * 16384 + (cu & 1) * 4096, sent4);
    { const int sl = tid >> 7, t = tid & 127; const u32 o = (t >> 5) * 2048 + j * 512 + (t & 31) * 16;
      stg128(ws, sl * QX_SLOT + o, QX_OFF + h * 16384, sent4); stg128(ws, sl * QX_SLOT + o, QX_OFF + h * 16384 + 8192, sent4); }
    stg128(ws, (tid >> 7) * MX_SLOT + ((tid >> 4) & 7) * 32768 + ((tid >> 3) & 1) * 8192 + (tid & 7) * 16, MX_OFF + (cu >> 6) * 16384 + (cu & 63) * 128, sent4);
    const bool rep = j == 0;
    if (rep && tid < 4) stg32(ws, tid * SX_SLOT, SX_OFF + h * 128, SENT);
    if (tid < 32) *(LAS u32*)(L + L_TS + tid * 4) = 0u;
    if (w == 7 && !(a.flags & 32)) {
        u32 v = ldg32(ws, (lane & 31) * 65536 + 24576 + 4096 * j + 256, KXF_OFF + h * XH);
        asm volatile("" :: "v"(v));
    }
    u32x4 w2[16];
    auto request2 = [&]() {
#pragma unroll
        for (int i = 0; i < 16; ++i) {
            const int piece = i * 8 + w;
            const u16* src = piece < 64 ? a.Wv + (size_t)(piece * 16 + (lane >> 2)) * 4096 + h * 128 + j * 32 + (lane & 3) * 8
                                        : a.Wo + (size_t)(h * 128 + (piece - 64) * 2 + (lane >> 5)) * 1024 + j * 256 + (lane & 31) * 8;
            w2[i] = *(const u32x4*)src;
        }
    };
    auto stage2 = [&]() {
        LBAR();
#pragma unroll
        for (int i = 0; i < 16; ++i) *(LAS u32x4*)(L + (i * 8 + w) * 1024 + lane * 16) = w2[i];
        LBAR();
    };
    auto stage1 = [&]() {
        const u32x4 srq = mk_srd(a.Wq, 1024 * 8192), srk = mk_srd(a.Wk, 1024 * 8192);
#pragma unroll
        for (int i = 0; i < 16; ++i) {
            const int piece = i * 8 + w;
            const int mat = i >> 3;
            dma16(mat ? srk : srq, (u32)(size_t)L + piece * 1024, (lane >> 2) * 8192 + (lane & 3) * 16, (piece & 63) * 16 * 8192 + h * 256 + j * 64);
        }
        SCHED();
        request2();
        SCHED();
        asm volatile("s_waitcnt vmcnt(16)" ::: "memory");
        LBAR();
        PST(0);
        if (tid == 0) stg32(ws, 0, RDY_OFF + cu * 128, ((u32)__builtin_amdgcn_s_getreg(20 | (3 << 11)) >> 2) & 1u);
    };
    const int qt = j & 1, eh = j >> 1;
    auto stage3 = [&]() {
        LBAR();
        PST(1);
#pragma unroll
        for (int i = 0; i < 2; ++i) { const int u = i * 512 + tid; *(LAS u32x4*)(L + L_M + u * 16) = *(const u32x4*)(a.mem0 + (u & 7) * 1024 + (u >> 3) * 8); }
        if (!(a.flags & 4)) poll_words(ws, XF_OFF + h * 128, 3, lane, cap, 2);
        PST(2);
        const u32x4 kx = ldg(ws, tid * 16, KXF_OFF + h * XH), vx = ldg(ws, tid * 16, VXF_OFF + h * XH);
        *(LAS u32x4*)(L + L_KX + tid * 16) = kx; *(LAS u32x4*)(L + L_VX + tid * 16) = vx;
        if (w < 4) *(LAS u32x4*)(L + L_QX + tid * 16) = ldg(ws, tid * 16, QXF_OFF + h * XH + qt * 4096);
        const u32x4 kx1 = ldg(ws, tid * 16, KXF_OFF + h * XH + XS), vx1 = ldg(ws, tid * 16, VXF_OFF + h * XH + XS);
        *(LAS u32x4*)(L + L_XB + L_KX + tid * 16) = kx1; *(LAS u32x4*)(L + L_XB + L_VX + tid * 16) = vx1;
        if (w < 4) *(LAS u32x4*)(L + L_XB + L_QX + tid * 16) = ldg(ws, tid * 16, QXF_OFF + h * XH + XS + qt * 4096);
        LBAR();
        if (w == 7) {
            for (u32 sp = 0;; ++sp) {
                MEMFENCE();
                const u32 r0 = ldg32(ws, lane * 128, RDY_OFF), r1 = ldg32(ws, lane * 128, RDY_OFF + 8192);
                const u32 nb = ldg32(ws, (lane & 3) * 128, NEAR_OFF);
                const unsigned long long s0 = __ballot(r0 & 1), s1 = __ballot(r1 & 1), nn = __ballot(nb & 1);
                if (lane == 0) { *(LAS u32x4*)(L + L_TS + 32) = u32x4{(u32)s0, (u32)(s0 >> 32), (u32)s1, (u32)(s1 >> 32)}; *(LAS u32*)(L + L_TS + 48) = (u32)nn & 15u; }
                if (__all(r0 != SENT && r1 != SENT && nb != SENT)) break;
                if (sp > cap) { give_up(ws, 1); cap = 0; break; }
                __builtin_amdgcn_s_sleep(8);
            }
        }
        LBAR();
        PST(3);
    };
    const int idx = (l16 & 8) ? 0xEEEE : 0x4444;
    const f32x4 z4 = {0.f, 0.f, 0.f, 0.f};
    constexpr float SCL2 = 0.08838834764831845f * 1.4426950408889634f;
    u32 tref = now32() - 100000u;
    int mq = (a.e1 + a.d1) | ((a.e1 + a.d1 + a.e2 + a.d2) << 10) | ((a.e1 + a.d1 + a.e2 + a.d2 + a.e3 + a.d3 + a.mq0) << 20);
#define O1 (mq & 1023)
#define O2 ((mq >> 10) & 1023)
#define OP ((u32)mq >> 20)
    auto note_fail = [&](u32 bit) { if (lane == 0) __hip_atomic_fetch_or((LAS u32*)(L + L_TS + 68), bit, __ATOMIC_RELAXED, __HIP_MEMORY_SCOPE_WORKGROUP); };
    u32 l16b = lane * 16;
    PIN(l16b);
    LAS char* const mwr = L + L_M + w * 2048 + l16b;
    u32 npage = 0, mrep = 0;
    auto side = [&](int c) { return (*(LAS u32*)(L + L_TS + 32 + (c >> 5) * 4) >> (c & 31)) & 1u; };
    auto get_near = [&]() {
        npage = __builtin_amdgcn_readfirstlane(*(LAS u32*)(L + L_TS + 48));
        mrep = MX_OFF + (cu & 3) * 32768 + __builtin_amdgcn_readfirstlane(((npage >> 3) ^ side(cu & 3)) & 1) * 8192;
    };
    auto land = [&](int s, u32x4 x, u32x4 y, u32 o) {
        const u32 mo = mrep + (s & 3) * MX_SLOT + o + (o & 8192) + a.slack * 131072;
        LAS char* dst = L + L_M + o + l16b;
        *(LAS u32x4*)dst = x; *(LAS u32x4*)(dst + 1024) = y;
        if (__builtin_expect(__any(max(umax4(x), umax4(y)) == SENT), 0)) {
            bool n0 = umax4(x) == SENT, n1 = umax4(y) == SENT;
            note_fail(1);
            for (u32 sp = 0; __any(n0 || n1); ++sp) {
                FAIL(10);
                if (sp > cap) { give_up(ws, 0x100 + s); cap = 0; break; }
                MEMFENCE();
                if (n0) { x = ldg(ws, l16b, mo); *(LAS u32x4*)dst = x; n0 = umax4(x) == SENT; }
                if (n1) { y = ldg(ws, l16b + 1024, mo); *(LAS u32x4*)(dst + 1024) = y; n1 = umax4(y) == SENT; }
            }
        }
    };
    auto rd = [&](LAS char* p) { const u32x2 x0 = trrd(p), x1 = trrd(p + 256); return u32x4{x0.x, x0.y, x1.x, x1.y}; };

    if (w < 4) {
        u32x8 wq[16], wv[8];
        const int t = w & 1, kh = w >> 1;
        const int wrow = 16 * (g >> 1) + 8 * (l16 >> 3) + 4 * (g & 1) + ((l16 >> 2) & 1);
        stage1();
        {
            LAS char* base = L + (w >> 1) * 65536 + wrow * 64 + t * 32 + (l16 & 3) * 8;
            LAS char* bA = base + (1 - kh) * 32768, *bB = base + kh * 32768;
#pragma unroll
            for (int ks = 0; ks < 16; ++ks)
#pragma unroll
                for (int q = 0; q < 4; ++q) { const u32x2 v = trrd((ks < 8 ? bA : bB) + (64 * (ks & 7) + 32 * (q >> 1) + 2 * (q & 1)) * 64); wq[ks][2 * q] = v.x; wq[ks][2 * q + 1] = v.y; }
        }
        stage2();
        {
            LAS char* base = L + (512 * kh + wrow) * 64 + t * 32 + (l16 & 3) * 8;
#pragma unroll
            for (int ks = 0; ks < 8; ++ks)
#pragma unroll
                for (int q = 0; q < 4; ++q) { const u32x2 v = trrd(base + (64 * ks + 32 * (q >> 1) + 2 * (q & 1)) * 64); wv[ks][2 * q] = v.x; wv[ks][2 * q + 1] = v.y; }
        }
        stage3();
        get_near();
        const u32 qx0 = QX_OFF + h * 16384 + __builtin_amdgcn_readfirstlane(((npage >> 2) ^ side(h)) & 1) * 8192;
        const u32 myside = __builtin_amdgcn_readfirstlane(side(cu));
        const u32 mreset = (lane >> 3) * 32768 + (((npage >> 3) ^ side((lane >> 3) & 3)) & 1) * 8192 + (lane & 7) * 16;
        LAS char* const mrd = L + L_M + l16b;
        LAS char* const mA = mrd + (1 - kh) * 8192, * const mB = mrd + kh * 8192;
        const u32 pub = (w >> 1) * 2048 + (32 * j + 16 * t) * 16;
        int e3 = a.e3 * 8, l1 = a.d1, l3 = a.d3;
        bool was_late = false;
        int xw = 2;
#pragma unroll 1
        for (int s = 0; s < NSEG; ++s) {
            const u32 qx = qx0 + (s & 3) * QX_SLOT;
            TSZERO;
            f32x4 c0 = z4, e0 = z4;
            if (s > 0) {
                wait_sched(tref + O1, 0);
                TS(0);
                MEMFENCE();
                const u32 mo = mrep + (s & 3) * MX_SLOT + w * 2048;
                const u32x4 v0 = ldg(ws, l16b, mo), v1 = ldg(ws, l16b + 1024, mo);
                PIN(c0); PIN(e0);
                land(s, v0, v1, w * 2048);
            }
            TS(1);
            LBAR();
            TS(2);
            u32x4 af[8];
#pragma unroll
            for (int ks = 0; ks < 7; ++ks) af[ks] = *(LAS u32x4*)(mA + ks * 1024);
            SCHED();
#pragma unroll
            for (int ks = 0; ks < 16; ++ks) {
                const int nx = ks + 7;
                c0 = smf(af[ks & 7], wq[ks], c0, idx);
                if (nx < 16) af[nx & 7] = *(LAS u32x4*)(nx < 8 ? mA + nx * 1024 : mB + (nx - 8) * 1024);
                SCHED();
            }
            TS(3);
            e0 = smf(af[0], wv[0], e0, idx); e0 = smf(af[1], wv[1], e0, idx);
            SCHED();
            { const u32x4 v = widen(min(pk(hadd32(c0[0], c0[2]), hadd32(c0[1], c0[3])), SENT - 1)); if (l16b < 256) stg128(ws, l16b, qx + pub, v); }
            SCHED();
            TS(4);
#pragma unroll
            for (int ks = 2; ks < 8; ++ks) e0 = smf(af[ks], wv[ks], e0, idx);
            { const u32x4 v = widen(min(pk(hadd32(e0[0], e0[2]), hadd32(e0[1], e0[3])), SENT - 1)); if (l16b < 256) stg128(ws, l16b, qx + 4096 + pub, v); }
            TS(5);
            const u32 tqk = now32();
            sleep_from(tqk, max((int)(tref + O2 - tqk), a.m2));
            TS(6);
            u32x4 qk = ldg(ws, l16b, qx + w * 1024);
            nap(a.m2v);
            u32x4 vv = ldg(ws, l16b, qx + 4096 + w * 1024);
            SCHED();
            auto land1 = [&](u32x4 x, u32 o, u32 bit) {
                LAS char* dst = mwr + (L_QK - L_M) - w * 1024 + o;
                *(LAS u32x4*)dst = x;
                if (__builtin_expect(!__any(umax4(x) == SENT), 1)) return false;
                note_fail(bit);
                bool n = umax4(x) == SENT;
                for (u32 sp = 0; __any(n); ++sp) {
                    FAIL(o ? 12 : 11);
                    if (sp > cap) { give_up(ws, 0x200 + s); cap = 0; break; }
                    MEMFENCE();
                    if (n) { x = ldg(ws, l16b, qx + o + w * 1024); *(LAS u32x4*)dst = x; n = umax4(x) == SENT; }
                }
                return true;
            };
            land1(qk, 0, 2);
            TS(7);
            LBAR();
            TS(8);
            unsigned long long tb2 = 0;
            const bool stamper = rep && w == 0;
            if (stamper) asm volatile("s_memrealtime %0" : "=s"(tb2) :: "memory");
            if (land1(vv, 4096, 8) && stamper) asm volatile("s_memrealtime %0" : "=s"(tb2) :: "memory");
            const u32 sx = SX_OFF + (s & 3) * SX_SLOT;
            if (stamper) {
                asm volatile("s_waitcnt lgkmcnt(0)" : "+s"(tb2) :: "memory");
                const u32x2 lw = *(LAS u32x2*)(L + L_TS + 64);
                if (l16b == 0) { stg128(ws, 0, sx + h * 128, u32x4{min((u32)tb2, SENT - 1), tqk, lw.x, lw.y}); *(LAS u32*)(L + L_TS + 68) = 0u; }
            }
            LBAR();
            u32x4 pf[5];
            {
                const int sn = min(s + 2, NSEG - 1);
                const u32 src = h * XH + sn * XS;
                pf[0] = ldg(ws, l16b, KXF_OFF + src + w * 2048); pf[1] = ldg(ws, l16b + 1024, KXF_OFF + src + w * 2048); pf[2] = ldg(ws, l16b, VXF_OFF + src + w * 2048); pf[3] = ldg(ws, l16b + 1024, VXF_OFF + src + w * 2048);
                pf[4] = ldg(ws, l16b, QXF_OFF + src + qt * 4096 + w * 1024);
            }
            SCHED();
            if (s > 0) {
                const int sl = (s - 1) & 3;
                u32x4 sn = sent4; PIN(sn);
                stg128(ws, l16b, PX_OFF + sl * PX_SLOT + (cu >> 1) * 16384 + (((npage >> (sl >> 1)) ^ myside) & 1) * 8192 + (cu & 1) * 4096 + w * 1024, sn);
                const u32 qb = qx0 + sl * QX_SLOT;
                if (l16b < 512) stg128(ws, l16b, qb + w * 2048 + j * 512, sn);
                if (w == 3) {
                    stg128(ws, mreset, MX_OFF + sl * MX_SLOT + (cu >> 6) * 16384 + (cu & 63) * 128, sn);
                    if (rep && l16b == 0 && s > 1) stg32(ws, 0, SX_OFF + ((s - 2) & 3) * SX_SLOT + h * 128, sn.x);
                }
            }
            if (w == 1) {
                wait_sched(tref + O2 + a.d2v, a.pa);
                u32x4 t = sent4;
                for (u32 sp = 0;; ++sp) {
                    MEMFENCE();
                    if (t.x == SENT) t = ldg(ws, (l16b & 496) * 8, sx);
                    if (__builtin_expect(!__any(t.x == SENT), 1)) break;
                    if (sp > 4096) break;
                }
                const int x3 = __builtin_amdgcn_readfirstlane(wave_max((int)(t.x - tref)));
                u32 an = tref + x3 + l3;
                const bool late = (int)(an - (tref + OP)) > a.wd1;
                if (late) { an += a.d3l; FAIL(13); } else an = tref + OP;
                if (l16b == 0) *(LAS u32x2*)(L + L_TS) = u32x2{an, (u32)s + 1};
                const int x1 = __builtin_amdgcn_readfirstlane(wave_max((int)(t.z - tref)));
                if (s > 1) {
                    if (x1 <= a.e1 + 2 && __popcll(__ballot(t.w & 1)) >= 8) l1 = min(l1 + a.mup, a.d1 + 12);
                    if (!was_late && __popcll(__ballot(t.w & 4)) >= 4) l3 = min(l3 + a.mup, a.d3 + 10);
                    const int x = (x3 - O2) * 8;
                    if (x <= e3) e3 = max(e3 - min(e3 - x, a.mdn), 160);
                    else e3 = min(e3 + min(x - e3, a.mmax), 2000);
                }
                was_late = late;
                const int n1 = a.e1 + l1, n2 = n1 + a.e2 + a.d2, np = n2 + (e3 >> 3) + l3 + a.mq0;
                if (l16b == 0) *(LAS u32x2*)(L + L_TS + 8) = u32x2{(u32)(n1 | (n2 << 10) | (np << 20)), (u32)s + 1};
            }
            {
                LAS char* dst = mwr - L_M + xw * L_XB;
                *(LAS u32x4*)(dst + L_KX) = pf[0]; *(LAS u32x4*)(dst + L_KX + 1024) = pf[1]; *(LAS u32x4*)(dst + L_VX) = pf[2]; *(LAS u32x4*)(dst + L_VX + 1024) = pf[3];
                *(LAS u32x4*)(dst + L_QX - w * 1024) = pf[4];
                xw = xw == 2 ? 0 : xw + 1;
            }
            if (s < NSEG - 1) wait_sched(tref + OP + O1 - 16, 0);
            tref = get_anchor(L + L_TS, s, mq);
            TS(9); TSUM(10);
        }
    } else {
        const int ww = w - 4;
        u32x8 wo[8];
        stage1();
        stage2();
        {
            const int jj = l16 >> 2;
            LAS char* b2 = L + 65536 + (16 * (jj >> 1) + 32 * (g & 1) + 4 * (g >> 1) + (jj & 1)) * 512 + ww * 128 + (l16 & 3) * 16;
#pragma unroll
            for (int ct = 0; ct < 4; ++ct)
#pragma unroll
                for (int ks = 0; ks < 2; ++ks)
#pragma unroll
                    for (int m = 0; m < 4; ++m) { const u32x2 v = trrd(b2 + (64 * ks + 8 * (m >> 1) + 2 * (m & 1)) * 512 + (ct & 1) * 64 + (ct >> 1) * 8); wo[ct * 2 + ks][2 * m] = v.x; wo[ct * 2 + ks][2 * m + 1] = v.y; }
        }
        stage3();
        __builtin_amdgcn_s_setprio(2);
        LAS char* const vrd = L + L_VM + (g >> 1) * 2048 + l16 * 16 + (g & 1) * 8;
        LAS char* const qrd = L + L_QK + (4 * g + (l16 >> 2)) * 16 + (l16 & 1) * 8;
        get_near();
        const int red = 32 * j + 8 * ww + 4 * (lane >> 5) + (l16 >> 2);
        const u32 pubp = (red >> 1) * 16384 + (red & 1) * 4096 + side(red) * 8192 + h * 128 + ((g & 1) * 4 + (l16 & 3)) * 16;
        const u32 pin0 = (cu >> 1) * 16384 + (cu & 1) * 4096 + __builtin_amdgcn_readfirstlane(side(cu)) * 8192;
        const int mr = (l16 & 1) + 2 * (l16 >> 3);
        const u32 pubm = ((lane >> 5) * 4 + mr) * 32768 + (((npage >> 3) ^ side(mr)) & 1) * 8192 + (cu >> 6) * 16384 + (cu & 63) * 128 + (4 * (g & 1) + ((l16 >> 1) & 3)) * 16;
        u32x4 sel;
        { const u32 e = 4 * (l16 & 1) + ((l16 >> 1) & 3), one = (e & 1) ? 0x3F800000u : 0x3F80u; sel = u32x4{(e >> 1) == 0 ? one : 0u, (e >> 1) == 1 ? one : 0u, (e >> 1) == 2 ? one : 0u, (e >> 1) == 3 ? one : 0u}; }
        u32 mlo = (l16 & 8) ? 0u : ~0u;
        u32x4 halves = {0x3F003F00u, 0x3F003F00u, 0x3F003F00u, 0x3F003F00u};
        PIN(halves); PIN(mlo);
        int xr = 0;
#pragma unroll 1
        for (int s = 0; s < NSEG; ++s) {
            const u32 px_ = PX_OFF + (s & 3) * PX_SLOT;
            LAS char* const xb = L + xr * L_XB + l16b;
            xr = xr == 2 ? 0 : xr + 1;
            u32x4 kx[8], vx[8];
            TSZERO; TS(0);
#pragma unroll
            for (int k = 0; k < 8; ++k) kx[k] = *(LAS u32x4*)(xb + L_KX + k * 1024);
#pragma unroll
            for (int k = 0; k < 8; ++k) vx[k] = *(LAS u32x4*)(xb + L_VX + k * 1024);
            if (w >= 6 && s > 0) {
                wait_sched(tref + O1 + a.m1, 0);
                MEMFENCE();
                const u32 o = (w - 4) * 4096, mo = mrep + (s & 3) * MX_SLOT + o + 8192;
                const u32x4 v0 = ldg(ws, l16b, mo), v1 = ldg(ws, l16b + 1024, mo), v2 = ldg(ws, l16b + 2048, mo), v3 = ldg(ws, l16b + 3072, mo);
                land(s, v0, v1, o); land(s, v2, v3, o + 2048);
            }
            LBAR();
            if (w == 7 && ((s + 2) & 7) == 0 && !(a.flags & 4)) poll_words(ws, XF_OFF + h * 128 + min((s + 2) >> 3, 7) * 12, 3, lane, cap, 0x600 + s);
            if (w == 6 && s > 0 && s - 1 < WO_DIRECT && lane == 0) count_up(a.ws, OAC_OFF + (h >> 1) * 256 + (s - 1) * 4);
            f32x4 d[4] = {z4, z4, z4, z4};
            const u32 pflip = ((npage >> ((s >> 1) & 1)) & 1) << 13;
            u32 po = pubp ^ pflip;
            PIN(d[0]); PIN(d[1]); PIN(d[2]); PIN(d[3]); PIN(po);
            TS(1);
            LBAR();
            TS(2);
            u32x4 qf[4], km[4];
            qf[0] = rd(qrd); qf[1] = rd(qrd + 512); km[0] = rd(qrd + 2048); qf[2] = rd(qrd + 1024); km[1] = rd(qrd + 2048 + 512); qf[3] = rd(qrd + 1536); km[2] = rd(qrd + 2048 + 1024);
            SCHED();
            km[3] = rd(qrd + 2048 + 1536);
            u32x4 vm[4];
            {
                typedef float f32x2 __attribute__((ext_vector_type(2)));
                f32x4 sm, s0, s1;
                s0 = mfma32(kx[0], qf[0], z4); s1 = mfma32(kx[4], qf[0], z4); SCHED();
                s0 = mfma32(kx[1], qf[1], s0); s1 = mfma32(kx[5], qf[1], s1); sm = mfma32(km[0], qf[0], z4); SCHED();
                s0 = mfma32(kx[2], qf[2], s0); s1 = mfma32(kx[6], qf[2], s1); sm = mfma32(km[1], qf[1], sm); SCHED();
                s0 = mfma32(kx[3], qf[3], s0); s1 = mfma32(kx[7], qf[3], s1); SCHED();
                sm = mfma32(km[2], qf[2], sm); sm = mfma32(km[3], qf[3], sm); SCHED();
                asm volatile("s_barrier" : "+v"(s0), "+v"(s1), "+v"(sm) :: "memory");
#pragma unroll
                for (int k = 0; k < 4; ++k) { const u32x2 x0 = *(LAS u32x2*)(vrd + k * 512), x1 = *(LAS u32x2*)(vrd + k * 512 + 256); vm[k] = u32x4{x0.x, x0.y, x1.x, x1.y}; }
                float mxv = mx3(mx3(mx3(s0[0], s0[1], s0[2]), s0[3], s1[0]), mx3(s1[1], s1[2], s1[3]), s0[0]);
                SCHED();
                mxv = mx3(mx3(mxv, sm[0], sm[1]), sm[2], sm[3]);
                { auto r = __builtin_amdgcn_permlane32_swap(fb(mxv), fb(mxv), false, false); mxv = mx2v(bf(r[0]), bf(r[1])); }
                { auto r = __builtin_amdgcn_permlane16_swap(fb(mxv), fb(mxv), false, false); mxv = mx2v(bf(r[0]), bf(r[1])); }
                const float nm = -mxv * SCL2;
                const f32x2 c2 = {SCL2, SCL2}, n2 = {nm, nm};
                auto ex2 = [&](float x, float y) { const f32x2 t = __builtin_elementwise_fma(f32x2{x, y}, c2, n2); return pk(__builtin_amdgcn_exp2f(t[0]), __builtin_amdgcn_exp2f(t[1])); };
                const u32x4 px = {ex2(s0[0], s0[1]), ex2(s0[2], s0[3]), ex2(s1[0], s1[1]), ex2(s1[2], s1[3])};
                const u32x4 pxl = {px.x & mlo, px.y & mlo, px.z & mlo, px.w & mlo};
                SCHED();
                f32x4 o[4];
                o[0] = mfma32(vx[0], pxl, z4); o[1] = mfma32(vx[2], pxl, z4);
                const u32x4 pxh = {px.x & ~mlo, px.y & ~mlo, px.z & ~mlo, px.w & ~mlo};
                SCHED();
                o[0] = mfma32(vx[1], pxh, o[0]); o[1] = mfma32(vx[3], pxh, o[1]);
                const u32 pm0 = ex2(sm[0], sm[1]);
                SCHED();
                o[2] = mfma32(vx[4], pxl, z4); o[3] = mfma32(vx[6], pxl, z4);
                const u32 pm1 = ex2(sm[2], sm[3]);
                const u32x4 pmm = {pm0 & mlo, pm1 & mlo, pm0 & ~mlo, pm1 & ~mlo};
                SCHED();
                o[0] = mfma32(vm[0], pmm, o[0]); o[1] = mfma32(vm[1], pmm, o[1]); SCHED();
                o[2] = mfma32(vx[5], pxh, o[2]); o[3] = mfma32(vx[7], pxh, o[3]); SCHED();
                o[2] = mfma32(vm[2], pmm, o[2]); o[3] = mfma32(vm[3], pmm, o[3]);
                const u32x4 af0 = pk8(o[0], o[1]);
                SCHED();
                d[0] = smf(af0, wo[0], d[0], idx); d[1] = smf(af0, wo[2], d[1], idx);
                PIN(d[0]); PIN(d[1]);
                f32x4 lx = mfma32(px, halves, z4), lm = mfma32(pmm, halves, z4);
                PIN(lx); PIN(lm);
                PIN(o[2]); PIN(o[3]);
                const u32x4 af1 = pk8(o[2], o[3]);
                d[0] = smf(af1, wo[1], d[0], idx); d[1] = smf(af1, wo[3], d[1], idx);
                PIN(d[0]); PIN(d[1]);
                d[2] = smf(af0, wo[4], d[2], idx); d[3] = smf(af0, wo[6], d[3], idx);
                PIN(lx);
                f32x4 inv;
#pragma unroll
                for (int i = 0; i < 4; ++i) inv[i] = __builtin_amdgcn_rcpf(fmaf(lx[i], 2.f, lm[i]));
                d[2] = smf(af1, wo[5], d[2], idx); d[3] = smf(af1, wo[7], d[3], idx);
                PIN(d[2]); PIN(d[3]);
                PIN(d[0]);
                const f32x4 r0 = f32x4{hadd32(d[0][0], d[1][0]), hadd32(d[0][1], d[1][1]), hadd32(d[0][2], d[1][2]), hadd32(d[0][3], d[1][3])} * inv;
                const f32x4 r1 = f32x4{hadd32(d[2][0], d[3][0]), hadd32(d[2][1], d[3][1]), hadd32(d[2][2], d[3][2]), hadd32(d[2][3], d[3][3])} * inv;
                const u32x4 pr = pk8(r0, r1);
                const u32x4 pv = {min(pr.x, SENT - 1), min(pr.y, SENT - 1), min(pr.z, SENT - 1), min(pr.w, SENT - 1)};
                stg128(ws, po, px_, pv);
            }
            TS(7);
            if (w == 4) {
                if (s < NSEG - 1) {
                    {
                        u32 t3, tn = tref + OP;
                        const u32 n = now32();
                        const bool have = peek_anchor(L + L_TS, s, t3);
                        if (!have && (int)(tn - n) < -a.e2) {
                            while (!peek_anchor(L + L_TS, s, t3)) __builtin_amdgcn_s_sleep(1);
                            wait_sched(t3, 0);
                        } else {
                            if (have) tn = t3;
                            sleep_from(n, max((int)(tn - n), a.m3));
                            if (!have && peek_anchor(L + L_TS, s, t3) && t3 != tn) wait_sched(t3, 0);
                        }
                    }
                    TS(3);
                    const u32 pin = px_ + (pin0 ^ pflip);
                    u32x4 v[4];
                    MEMFENCE();
#pragma unroll
                    for (int k = 0; k < 4; ++k) v[k] = ldg(ws, l16b + k * 1024, pin);
                    auto sum32 = [&]() {
                        f32x4 t = mfma32(v[0], sel, z4); t = mfma32(v[1], sel, t); t = mfma32(v[2], sel, t); t = mfma32(v[3], sel, t);
#pragma unroll
                        for (int i = 0; i < 4; ++i) { const auto r = __builtin_amdgcn_permlane32_swap(fb(t[i]), fb(t[i]), false, false); t[i] = bf(r[0]) + bf(r[1]); }
                        return t;
                    };
                    f32x4 t = sum32();
                    if (__builtin_expect(__any(__builtin_isunordered(t[0], t[1]) || __builtin_isunordered(t[2], t[3])), 0)) {
                        for (u32 sp = 0;; ++sp) {
                            bool bad = false;
                            MEMFENCE();
#pragma unroll
                            for (int k = 0; k < 4; ++k) if (umax4(v[k]) == SENT) { bad = true; v[k] = ldg(ws, l16b + k * 1024, pin); }
                            if (!__any(bad)) break;
                            FAIL(12); note_fail(4);
                            if (sp > cap) { give_up(ws, 0x500 + s); cap = 0; break; }
                        }
                        t = sum32();
                    }
                    {
                        const u32 lo = min(pk(t[0], t[1]), SENT - 1), hi = min(pk(t[2], t[3]), SENT - 1);
                        const u32 x = __builtin_amdgcn_update_dpp(0u, lo, 0xB1, 0xf, 0xf, true), y = __builtin_amdgcn_update_dpp(0u, hi, 0xB1, 0xf, 0xf, true);
                        const bool odd = lane & 1;
                        stg128(ws, pubm, MX_OFF + ((s + 1) & 3) * MX_SLOT, u32x4{odd ? x : lo, odd ? y : hi, odd ? lo : x, odd ? hi : y});
                    }
                    { const u32 tm = now32(); if (lane == 0) *(LAS u32*)(L + L_TS + 64) = tm; }
                }
            } else if (w == 5 && !(a.flags & 1)) {
                f32x4 sm = z4, s0 = z4, s1 = z4;
#pragma unroll
                for (int ks = 0; ks < 4; ++ks) { const u32x4 qq = *(LAS u32x4*)(xb + L_QX + ks * 1024); sm = mfma32(km[ks], qq, sm); s0 = mfma32(kx[ks], qq, s0); s1 = mfma32(kx[4 + ks], qq, s1); }
                const int rq = 16 * qt + l16;
                constexpr float MASKV = -1e9f / 0.08838834764831845f;
#pragma unroll
                for (int i = 0; i < 4; ++i) { if (4 * g + i > rq) s0[i] = MASKV; if (16 + 4 * g + i > rq) s1[i] = MASKV; }
                const float nm = -rowmax(s0, s1, sm) * SCL2;
#pragma unroll
                for (int i = 0; i < 4; ++i) { sm[i] = __builtin_amdgcn_exp2f(fmaf(sm[i], SCL2, nm)); s0[i] = __builtin_amdgcn_exp2f(fmaf(s0[i], SCL2, nm)); s1[i] = __builtin_amdgcn_exp2f(fmaf(s1[i], SCL2, nm)); }
                const u32 pm0 = pk(sm[0], sm[1]), pm1 = pk(sm[2], sm[3]);
                const u32x4 pe = {pm0, pm1, 0u, 0u}, po = {0u, 0u, pm0, pm1};
                const u32x4 px = {pk(s0[0], s0[1]), pk(s0[2], s0[3]), pk(s1[0], s1[1]), pk(s1[2], s1[3])};
                const f32x4 lx = mfma32(halves, px, z4), lm = mfma32(halves, pe, z4);
                f32x4 o[4];
#pragma unroll
                for (int t = 0; t < 4; ++t) {
                    const u32x4 vxt = eh ? vx[4 + t] : vx[t], vmt = eh ? vm[2 + (t >> 1)] : vm[t >> 1];
                    o[t] = mfma32(vxt, px, z4); o[t] = mfma32(vmt, (t & 1) ? po : pe, o[t]);
                }
                const float inv = __builtin_amdgcn_rcpf(fmaf(lx[0], 2.f, lm[0]));
#pragma unroll
                for (int p = 0; p < 2; ++p) {
                    const f32x4 x = o[2 * p] * inv, y = o[2 * p + 1] * inv;
                    stg128(ws, l16b + p * 1024, OAF_OFF + h * XH + s * XS + qt * 4096 + eh * 2048, u32x4{pk(x[0], x[1]), pk(x[2], x[3]), pk(y[0], y[1]), pk(y[2], y[3])});
                }
                DRAIN();
                if (s >= WO_DIRECT && lane == 0) count_up(a.ws, OAC_OFF + (h >> 1) * 256 + s * 4);
            }
            TS(8);
            TS(9);
            if (w >= 6 && s < NSEG - 1) wait_sched(tref + OP + O1 - 30, 0);
            tref = get_anchor(L + L_TS, s, mq);
        }
    }
    TSOUT;
}

__device__ __forceinline__ void wo_role(const Args& a, LAS char* L, const rsrc_t ws, const int id) {
    const int tid = threadIdx.x, lane = tid & 63, w = __builtin_amdgcn_readfirstlane(tid >> 6);
    const int kh = (id >> 1) & 1, nb = (id & 1) + 2 * (id >> 2), g = lane >> 4, l16 = lane & 15;
    u32 cap = SPIN_CAP;
    u32x4 wr[8][4];
    if (id < 4 && w == 0) {
        const u32x4 sn = {SENT, SENT, SENT, SENT};
        const u32 base = id < 2 ? PX_OFF + id * 0x200000 : (id == 2 ? QX_OFF : MX_OFF);
        stg128(ws, (lane >> 5) * 8192 + (lane & 31) * 16, base, sn); stg128(ws, (lane >> 5) * 8192 + 512 + (lane & 31) * 16, base, sn);
        DRAIN();
        u32 best[2] = {~0u, ~0u};
#pragma unroll
        for (int i = 0; i < 12; ++i) {
            const unsigned long long t0 = __builtin_amdgcn_s_memtime();
            u32 v = ldg32(ws, 0, base + (i & 1) * 8192 + (i >> 1) * 128);
            asm volatile("s_waitcnt vmcnt(0)" : "+v"(v) :: "memory");
            best[i & 1] = min(best[i & 1], (u32)(__builtin_amdgcn_s_memtime() - t0));
        }
        stg128(ws, (lane >> 5) * 8192 + (lane & 31) * 16, base, sn); stg128(ws, (lane >> 5) * 8192 + 512 + (lane & 31) * 16, base, sn);
        DRAIN();
        if (lane == 0) stg32(ws, 0, NEAR_OFF + id * 128, ((best[1] < best[0] ? 1u : 0u) ^ ((u32)__builtin_amdgcn_s_getreg(20 | (3 << 11)) >> 2) ^ ((u32)a.flags >> 6)) & 1u);
    }
    if (a.flags & (4 | 128)) return;
    if (id < 4) LBAR();
    if (a.flags & 4096) {
        u32 sum = 0;
        sum += *(const u32*)(a.Ax + ((id >> 2) * 512 + tid) * 64);
#pragma unroll
        for (int i = 0; i < 12; ++i) {
            const int it = i * 8 + w, gi = (id & 3) + 4 * (3 * (id >> 2) + it % 3);
            const u16* Wp = gi < 32 ? a.Wk : (gi < 64 ? a.Wv : a.Wq);
            sum += *(const u32*)(Wp + (size_t)((it / 3) * 32 + (lane >> 1)) * 4096 + (gi & 31) * 128 + (lane & 1) * 64);
        }
        asm volatile("" :: "v"(sum));
    }
    for (int i = 0; i < a.wsl; ++i) __builtin_amdgcn_s_sleep(64);
    if (!(a.flags & 65536)) {
        u32 sum = 0;
        const int t = (id * 512 + tid) * 64;
#pragma unroll
        for (int i = 0; i < 2; ++i) sum += *(const u32*)(a.Ax + i * 1048576 + t);
        asm volatile("" : "+v"(sum));
#pragma unroll
        for (int i = 0; i < 2; ++i) sum += *(const u32*)(a.Bx + i * 1048576 + t);
#pragma unroll
        for (int i = 0; i < 3; ++i) sum += *(const u32*)(a.wqkv + i * 1048576 + t);
        sum += *(const u32*)(a.wo + t);
        asm volatile("" :: "v"(sum));
    }
#pragma unroll
    for (int rnd = 0; rnd < 2; ++rnd) {
#pragma unroll
        for (int i = 0; i < 16; ++i) {
            const int piece = i * 8 + w;
            const u16* src = a.Wo + (size_t)(2048 * kh + 1024 * rnd + piece * 8 + (lane >> 3)) * 1024 + nb * 64 + (lane & 7) * 8;
            __builtin_amdgcn_global_load_lds((const u32*)src, (LAS u32*)(L + piece * 1024), 16, 0, 0);
        }
        DRAIN(); LBAR();
        if ((w >> 2) == rnd) {
            LAS char* base = L + ((w & 3) * 256 + 4 * g + (l16 >> 2)) * 128 + (l16 & 3) * 8;
#pragma unroll
            for (int ks = 0; ks < 8; ++ks)
#pragma unroll
                for (int ct = 0; ct < 4; ++ct) { LAS char* p = base + ks * 4096 + ct * 32; const u32x2 x0 = trrd(p), x1 = trrd(p + 2048); wr[ks][ct] = u32x4{x0.x, x0.y, x1.x, x1.y}; }
        }
        LBAR();
    }
    const f32x4 z4 = {0.f, 0.f, 0.f, 0.f};
    u32 l16b = lane * 16; PIN(l16b);
    if (kh == 0) {
        const u32x4 sn = {SENT, SENT, SENT, SENT};
#pragma unroll 4
        for (int s = 0; s < NSEG; ++s) stg128(ws, l16b, WP_OFF + (s * 16 + nb) * 8192 + w * 1024, sn);
        DRAIN(); LBAR();
        if (tid == 0) stg32(ws, 0, WF_OFF + nb * 256, 0u);
    } else poll_words(ws, WF_OFF + nb * 256, 1, lane, cap, 0x800);
    f32x4 keep = z4;
    u32 tp = 0;
    u16* const outp = a.out + (size_t)(16 * (w >> 2) + l16) * 2048 + nb * 64 + 16 * (w & 3) + 4 * g;
    const rsrc_t ro = __builtin_amdgcn_make_buffer_rsrc(a.out, 0, 2048 * 4096, 0x00020000);
    const u32 outo = (16 * (w >> 2) + l16) * 4096 + (nb * 64 + 16 * (w & 3) + 4 * g) * 2;
    auto finish = [&](int s, u32x4 p) {
        for (u32 sp = 0; __any(umax4(p) == SENT); ++sp) {
            if (sp > cap) { give_up(ws, 0x801 + s); cap = 0; break; }
            MEMFENCE();
            p = ldg(ws, l16b, WP_OFF + (s * 16 + nb) * 8192 + w * 1024);
        }
        const f32x4 r = keep + __builtin_bit_cast(f32x4, p);
        if (a.stag & 1) __builtin_amdgcn_raw_buffer_store_b64(u32x2{pk(r[0], r[1]), pk(r[2], r[3])}, ro, outo, s * 32 * 4096, SC1);
        else *(u32x2*)(outp + (size_t)s * 32 * 2048) = u32x2{pk(r[0], r[1]), pk(r[2], r[3])};
    };
    const u32 cnt = OAC_OFF + (8 * kh + w) * 256;
    auto await = [&](int s) {
        if (s > 0 && s < WO_DIRECT && (u32)(tp - now32()) < (u32)a.per) wait_sched(tp, 0);
        for (u32 sp = 0;; ++sp) {
            MEMFENCE();
            const u32 v = ldg32(ws, 0, cnt + s * 4);
            const u32 t = ldg32(ws, 0, SX_OFF + (s & 3) * SX_SLOT);
            tp = __builtin_amdgcn_readfirstlane(t) + a.per + a.wd2;
            if (__builtin_amdgcn_readfirstlane(v) == 7u) break;
            if (sp > cap) { give_up(ws, 0x700 + s); cap = 0; break; }
            if (s < WO_DIRECT) __builtin_amdgcn_s_sleep(16); else __builtin_amdgcn_s_sleep(2);
        }
    };
    u32x4 x[2][8], pp = {0u, 0u, 0u, 0u};
    u32 la;
    auto ldx = [&](int s, int rt, int ks) { x[rt][ks] = ldg(ws, l16b + (ks & 3) * 1024, OAF_OFF + (16 * kh + 2 * w + (ks >> 2)) * XH + s * XS + rt * 4096); };
    auto look = [&](int s) { MEMFENCE(); la = ldg32(ws, 0, cnt + min(s + 1, NSEG - 1) * 4); };
    auto request = [&](int s) {
        look(s);
#pragma unroll
        for (int ks = 0; ks < 8; ++ks) { ldx(s, 0, ks); ldx(s, 1, ks); }
        SCHED();
    };
    await(0);
    request(0);
#pragma unroll 1
    for (int s = 0; s < NSEG; ++s) {
        if ((s & 3) == 0) PST(s >> 2);
        const bool pre = __builtin_amdgcn_readfirstlane(la) == 7u && s + 1 < NSEG;
        if (kh == 0 && s > 0) pp = ldg(ws, l16b, WP_OFF + ((s - 1) * 16 + nb) * 8192 + w * 1024);
        f32x4 acc[2][4];
#pragma unroll
        for (int rt = 0; rt < 2; ++rt)
#pragma unroll
            for (int ct = 0; ct < 4; ++ct) acc[rt][ct] = z4;
        if (pre) {
            look(s + 1);
#pragma unroll
            for (int ks = 0; ks < 8; ++ks) {
#pragma unroll
                for (int rt = 0; rt < 2; ++rt)
#pragma unroll
                    for (int ct = 0; ct < 4; ++ct) acc[rt][ct] = mfma32(wr[ks][ct], x[rt][ks], acc[rt][ct]);
                PIN(acc[1][3]);
                ldx(s + 1, 0, ks); ldx(s + 1, 1, ks);
                SCHED();
            }
            tp += a.per;
        } else {
#pragma unroll
            for (int ks = 0; ks < 8; ++ks)
#pragma unroll
                for (int rt = 0; rt < 2; ++rt)
#pragma unroll
                    for (int ct = 0; ct < 4; ++ct) acc[rt][ct] = mfma32(wr[ks][ct], x[rt][ks], acc[rt][ct]);
        }
        LAS char* red = L + (s & 1) * 65536;
#pragma unroll
        for (int rt = 0; rt < 2; ++rt)
#pragma unroll
            for (int ct = 0; ct < 4; ++ct) *(LAS f32x4*)(red + w * 8192 + (rt * 4 + ct) * 1024 + l16b) = acc[rt][ct];
        if (kh == 0 && s > 0) finish(s - 1, pp);
        LBAR();
        f32x4 t[8];
#pragma unroll
        for (int k = 0; k < 8; ++k) t[k] = *(LAS f32x4*)(red + k * 8192 + w * 1024 + l16b);
        keep = ((t[0] + t[1]) + (t[2] + t[3])) + ((t[4] + t[5]) + (t[6] + t[7]));
        if (kh == 1) stg128(ws, l16b, WP_OFF + (s * 16 + nb) * 8192 + w * 1024, __builtin_bit_cast(u32x4, keep));
        if (!pre && s + 1 < NSEG) { await(s + 1); request(s + 1); }
    }
    if (kh == 0) { nap(6); finish(NSEG - 1, u32x4{SENT, SENT, SENT, SENT}); }
}

#define LGKM0() __builtin_amdgcn_s_waitcnt(0xC07F)
constexpr int G_BUF = 49152, G_B = 32768;
struct Unit { u32x4 ra, rw; u32 aoff, woff, ldw2; bool perm; };
__device__ __forceinline__ void g_dma(const Unit& u, u32 l0, u32 va, u32 vb, int q, int kt, int b) {
    if (q < 4) dma16(u.ra, l0 + b * G_BUF + q * 8192, va, u.aoff + kt * 128 + q * 64 * 2048);
    else dma16(u.rw, l0 + b * G_BUF + G_B + (q - 4) * 8192, vb, u.woff + (kt * 64 + (q - 4) * 32) * u.ldw2);
}
__device__ __forceinline__ u32 g_va(int tid) { return (tid >> 3) * 2048 + (((tid & 7) ^ ((tid >> 3) & 7)) << 4); }
__device__ __forceinline__ u32 g_vb(int tid, u32 ldw2, bool perm) { const int rb = tid >> 4, src = perm ? 16 * ((rb >> 2) & 1) + 4 * (rb >> 3) + (rb & 3) : rb; return src * ldw2 + (((tid & 15) ^ (((rb & 3) | (((rb >> 3) & 1) << 2)) << 1)) << 4); }
__device__ __forceinline__ void gemm_prime(LAS char* L, const Unit& u) {
    const int tid = threadIdx.x;
    const u32 l0 = (u32)(size_t)L + __builtin_amdgcn_readfirstlane(tid >> 6) * 1024, va = g_va(tid), vb = g_vb(tid, u.ldw2, u.perm);
#pragma unroll
    for (int i = 0; i < 18; ++i) g_dma(u, l0, va, vb, i % 6, i / 6, i / 6);
}
template <bool SWAP>
__device__ __forceinline__ void gemm_tile(LAS char* L, const rsrc_t ws, const Unit& u, const u32 pflag, const bool p16, const bool has_nx, const Unit& nx, f32x4 (&acc)[4][4]) {
    const int tid = threadIdx.x, lane = tid & 63, w = __builtin_amdgcn_readfirstlane(tid >> 6);
    const int wm = w >> 1, wn = w & 1, g = lane >> 4, l16 = lane & 15;
    const u32 va = g_va(tid), vb = g_vb(tid, u.ldw2, u.perm);
    const u32 l0 = (u32)(size_t)L + w * 1024;
    LAS char* const ap0 = L + (64 * wm + l16) * 128 + ((g ^ (l16 & 7)) << 4);
    LAS char* const ap1 = L + (64 * wm + l16) * 128 + (((4 + g) ^ (l16 & 7)) << 4);
    const int q = l16 >> 2, f = q | ((g & 1) << 2);
    LAS char* bp[4];
#pragma unroll
    for (int n = 0; n < 4; ++n) bp[n] = L + G_B + (8 * g + q) * 256 + (((4 * wn + n) ^ f) << 5) + (l16 & 3) * 8;
    auto rdA1 = [&](int m, int b, int kk) { return *(LAS u32x4*)((kk ? ap1 : ap0) + b * G_BUF + m * 2048); };
    auto rdB1 = [&](int n, int b, int kk) { const u32x2 x0 = trrd(bp[n] + b * G_BUF + kk * 8192), x1 = trrd(bp[n] + b * G_BUF + kk * 8192 + 1024); return u32x4{x0.x, x0.y, x1.x, x1.y}; };
    auto half = [&](const u32x4 (&A)[4], const u32x4 (&B)[4], u32x4 (&An)[4], u32x4 (&Bn)[4], bool rd, int b, int kk, auto&& gap) {
#pragma unroll
        for (int i = 0; i < 16; ++i) {
            const int m = i >> 2, n = i & 3;
            acc[m][n] = SWAP ? mfma32(B[n], A[m], acc[m][n]) : mfma32(A[m], B[n], acc[m][n]);
            if (rd && i < 8) { if (i & 1) Bn[i >> 1] = rdB1(i >> 1, b, kk); else An[i >> 1] = rdA1(i >> 1, b, kk); }
            gap(i);
            SCHED();
        }
    };
#pragma unroll
    for (int m = 0; m < 4; ++m)
#pragma unroll
        for (int n = 0; n < 4; ++n) acc[m][n] = f32x4{0.f, 0.f, 0.f, 0.f};
    u32x4 A0[4], B0[4], A1[4], B1[4];
    if (pflag && p16) asm volatile("s_waitcnt vmcnt(28)\n\ts_barrier" ::: "memory"); else if (pflag) asm volatile("s_waitcnt vmcnt(20)\n\ts_barrier" ::: "memory"); else asm volatile("s_waitcnt vmcnt(12)\n\ts_barrier" ::: "memory");
#pragma unroll
    for (int i = 0; i < 4; ++i) { A0[i] = rdA1(i, 0, 0); B0[i] = rdB1(i, 0, 0); }
    auto step = [&](int kt, int b, int bn, int tail) {
        LGKM0();
        half(A0, B0, A1, B1, true, b, 1, [](int) {});
        if (tail >= 2) asm volatile("s_waitcnt vmcnt(0) lgkmcnt(0)\n\ts_barrier" ::: "memory");
        else if (b < 2 && tail == 0 && kt < 2 && pflag) { if (p16) asm volatile("s_waitcnt vmcnt(22) lgkmcnt(0)\n\ts_barrier" ::: "memory"); else asm volatile("s_waitcnt vmcnt(14) lgkmcnt(0)\n\ts_barrier" ::: "memory"); }
        else asm volatile("s_waitcnt vmcnt(6) lgkmcnt(0)\n\ts_barrier" ::: "memory");
        if (b == 2 && tail == 0 && kt == 2 && pflag && tid == 0) stg32(ws, 0, pflag, 0u);
        LGKM0();
        if (tail == 0) half(A1, B1, A0, B0, true, bn, 0, [&](int i) { if (i >= 4 && !(i & 1)) g_dma(u, l0, va, vb, (i - 4) >> 1, kt + 3, b); });
        else if (tail < 3) half(A1, B1, A0, B0, true, bn, 0, [](int) {});
        else if (has_nx) { const u32 vbn = g_vb(tid, nx.ldw2, nx.perm); half(A1, B1, A0, B0, false, 0, 0, [&](int i) { g_dma(nx, l0, va, vbn, i % 6, i / 6, i / 6); if (i >= 14) g_dma(nx, l0, va, vbn, i - 10, 2, 2); }); }
        else half(A1, B1, A0, B0, false, 0, 0, [](int) {});
    };
#pragma unroll 1
    for (int kt = 0; kt < 12; kt += 3) { step(kt, 0, 1, 0); step(kt + 1, 1, 2, 0); step(kt + 2, 2, 0, 0); }
    step(12, 0, 1, 0); step(13, 1, 2, 1); step(14, 2, 0, 2); step(15, 0, 1, 3);
}

template <int Z>
__device__ __forceinline__ void swa_tile(LAS char* kp, LAS char* vp, const u32x4 qf, const int nmask, const int lane, f32x4& o0, f32x4& o1) {
    const int g = lane >> 4, c = lane & 15;
    const f32x4 z4 = {0.f, 0.f, 0.f, 0.f};
    constexpr float SC = 0.17677669529663687f, SCL2 = SC * 1.4426950408889634f, MASKV = -1e9f / SC;
    f32x4 sc[17];
#pragma unroll
    for (int t = 0; t < 17; ++t) sc[t] = mfma32(*(LAS u32x4*)(kp + (Z + t) * 1024), qf, z4);
#pragma unroll
    for (int i = 0; i < 4; ++i) { if (4 * g + i <= c) sc[0][i] = MASKV; if (4 * g + i > c) sc[16][i] = MASKV; }
    if (nmask > 0) {
#pragma unroll
        for (int t = 0; t < 16; ++t) if (t < nmask) sc[t] = f32x4{MASKV, MASKV, MASKV, MASKV};
    }
    float m = mx3(sc[16][0], sc[16][1], mx3(sc[16][2], sc[16][3], sc[16][0]));
#pragma unroll
    for (int t = 0; t < 16; ++t) m = mx3(mx3(m, sc[t][0], sc[t][1]), sc[t][2], sc[t][3]);
    { auto r = __builtin_amdgcn_permlane32_swap(fb(m), fb(m), false, false); m = mx2v(bf(r[0]), bf(r[1])); }
    { auto r = __builtin_amdgcn_permlane16_swap(fb(m), fb(m), false, false); m = mx2v(bf(r[0]), bf(r[1])); }
    const float nm = -m * SCL2;
    u32x2 p[18];
    p[Z ? 0 : 17] = u32x2{0u, 0u};
#pragma unroll
    for (int t = 0; t < 17; ++t) {
        const float e0 = __builtin_amdgcn_exp2f(fmaf(sc[t][0], SCL2, nm)), e1 = __builtin_amdgcn_exp2f(fmaf(sc[t][1], SCL2, nm));
        const float e2 = __builtin_amdgcn_exp2f(fmaf(sc[t][2], SCL2, nm)), e3 = __builtin_amdgcn_exp2f(fmaf(sc[t][3], SCL2, nm));
        p[Z + t] = u32x2{pk(e0, e1), pk(e2, e3)};
    }
    const u32x4 ones = {0x3F803F80u, 0x3F803F80u, 0x3F803F80u, 0x3F803F80u};
    f32x4 ls = z4; o0 = z4; o1 = z4;
#pragma unroll
    for (int m2 = 0; m2 < 9; ++m2) {
        const u32x4 pb = {p[2 * m2].x, p[2 * m2].y, p[2 * m2 + 1].x, p[2 * m2 + 1].y};
        o0 = mfma32(*(LAS u32x4*)(vp + m2 * 2048), pb, o0);
        o1 = mfma32(*(LAS u32x4*)(vp + m2 * 2048 + 1024), pb, o1);
        ls = mfma32(ones, pb, ls);
    }
    const float inv = __builtin_amdgcn_rcpf(ls[0]);
    o0 *= inv; o1 *= inv;
}

__device__ __forceinline__ void acquire_agent() { __builtin_amdgcn_fence(__ATOMIC_ACQUIRE, "agent"); DRAIN(); }

__device__ __forceinline__ void gemm_role(const Args& a, LAS char* L, const rsrc_t ws, const int gi) {
    const int tid = threadIdx.x, lane = tid & 63, w = __builtin_amdgcn_readfirstlane(tid >> 6);
    const int wm = w >> 1, wn = w & 1;
    u32 cap = SPIN_CAP;
    const u32 l16b = lane * 16;
    const u32x4 wsr = mk_srd(a.ws, WS_BYTES);
    f32x4 acc[4][4];
    auto unit = [&](int r) {
        const bool pa = r < 8;
        const int t = gi + 96 * (r - 8), blk = pa ? r : t / 24, ct = pa ? gi & 31 : t % 24, mat = gi >> 5;
        const u16* Wp = pa ? (mat == 0 ? a.Wk : (mat == 1 ? a.Wv : a.Wq)) : a.wqkv;
        const u32 ldw2 = (a.flags & 1024) ? 256 : (pa ? 8192 : 6144);
        if (a.flags & 1024) return Unit{mk_srd(pa ? a.Ax : a.Bx, 2048 * 1024 * 2), mk_srd(Wp, 0x600000), (u32)blk * 256 * 2048, (u32)(ct % 24) * 262144, ldw2, false};
        return Unit{mk_srd(pa ? a.Ax : a.Bx, 2048 * 1024 * 2), mk_srd(Wp, 1024 * ldw2), (u32)blk * 256 * 2048, (u32)ct * 256, ldw2, false};
    };
    Unit cur = unit(0);
    gemm_prime(L, cur);
    u32 pflag = 0;
    bool p16 = false;
#pragma unroll 1
    for (int r = 0; r < 10; ++r) {
        const bool pa = r < 8;
        const int t = gi + 96 * (r - 8);
        const int blk = pa ? r : t / 24, ct = pa ? gi & 31 : t % 24, mat = pa ? gi >> 5 : ct >> 3;
        const bool isv = pa ? mat == 1 : mat == 2;
        const bool stream = r > 0 && r < 9;
        const Unit nx = unit(min(r + 1, 9));
        if (isv) {
            gemm_tile<false>(L, ws, cur, pflag, p16, stream, nx, acc);
            p16 = false;
            const u32 base = pa ? VXF_OFF + ct * XH + (blk * 8 + wm * 2) * XS + wn * 4096 : VB_OFF + (ct & 7) * 524288 + blk * 16384 + wm * 4096 + wn * 262144;
            const u32 sp_ = pa ? XS : 2048, sn1 = pa ? 2048 : 131072;
#pragma unroll
            for (int p = 0; p < 2; ++p)
#pragma unroll
                for (int n = 0; n < 4; ++n) stg128(ws, l16b, base + p * sp_ + (n >> 1) * sn1 + (n & 1) * 1024, pk8(acc[2 * p][n], acc[2 * p + 1][n]));
        } else {
            gemm_tile<true>(L, ws, cur, pflag, p16, stream, nx, acc);
            const u32 base = pa ? (mat == 0 ? KXF_OFF : QXF_OFF) + ct * XH + (blk * 8 + wm * 2) * XS + wn * 2048
                                : (mat == 0 ? QB_OFF : KB_OFF) + (ct & 7) * 524288 + blk * 16384 + wm * 4096 + wn * 262144;
            const u32 sm1 = pa ? XS : 2048, sm0 = pa ? 4096 : 1024, sn = pa ? 1024 : 131072;
#pragma unroll
            for (int m = 0; m < 4; ++m)
#pragma unroll
                for (int p = 0; p < 2; ++p) stg128(ws, l16b, base + (m >> 1) * sm1 + (m & 1) * sm0 + p * sn, pk8(acc[m][2 * p], acc[m][2 * p + 1]));
            p16 = pa && mat == 2 && (a.stag & 4);
            if (p16) {
#pragma unroll
                for (int i = 0; i < 8; ++i) stg128(ws, l16b, OAF_OFF + ct * XH + (blk * 8 + w) * XS + i * 1024, u32x4{0u, 0u, 0u, 0u});
            }
        }
        pflag = pa ? XF_OFF + ct * 128 + (blk * 3 + mat) * 4 : BF_OFF + (ct & 7) * 128 + (blk * 3 + mat) * 4;
        if (!stream) {
            DRAIN(); LBAR();
            if (tid == 0) stg32(ws, 0, pflag, 0u);
            pflag = 0;
            if (r == 0) gemm_prime(L, nx);
        }
        cur = nx;
        PST(r);
    }
#pragma unroll 1
    for (int u = gi; u < 256; u += 96) {
        const int blk = u >> 5, h = u & 31;
        if (w == 0) {
            const int b0 = max(blk - 1, 0);
            poll_words(ws, BF_OFF + (h >> 2) * 128 + b0 * 12, (blk - b0 + 1) * 3, lane, cap, 0x900 + u);
            acquire_agent();
        }
        LBAR();
#pragma unroll
        for (int i = 0; i < 4; ++i) {
            const int pc = i * 8 + w, half = pc >> 4;
            const u32 rel = (u32)(max(blk - 1 + half, 0) * 16 + (pc & 15)) * 1024;
            dma16(wsr, (u32)(size_t)L + pc * 1024, l16b, KB_OFF + h * 131072 + rel);
            dma16(wsr, (u32)(size_t)L + 32768 + pc * 1024, l16b, VB_OFF + h * 131072 + rel);
        }
        const u32x4 q0 = __builtin_amdgcn_raw_buffer_load_b128(ws, l16b, QB_OFF + h * 131072 + (blk * 16 + 2 * w) * 1024, 0);
        const u32x4 q1 = __builtin_amdgcn_raw_buffer_load_b128(ws, l16b + 1024, QB_OFF + h * 131072 + (blk * 16 + 2 * w) * 1024, 0);
        DRAIN(); LBAR();
        LAS char* kp = L + w * 2048 + l16b, *vp = L + 32768 + w * 2048 + l16b;
        const u32 ob = OB_OFF + (blk * 256 + 32 * w) * 2048 + h * 64;
        const u32 ov = (lane & 15) * 2048 + (lane >> 4) * 16;
        f32x4 o0, o1;
        swa_tile<0>(kp, vp, q0, blk ? 0 : 16 - 2 * w, lane, o0, o1);
        stg128(ws, ov, ob, pk8(o0, o1));
        swa_tile<1>(kp, vp, q1, blk ? 0 : 15 - 2 * w, lane, o0, o1);
        stg128(ws, ov, ob + 16 * 2048, pk8(o0, o1));
        DRAIN(); LBAR();
        if (tid == 0) count_up(a.ws, OBC_OFF + blk * 128);
        PST(10 + u / 96);
    }
    if (gi < 64) {
        const int blk = gi >> 3, nt = gi & 7;
        if (w == 0) { poll_count(ws, OBC_OFF + blk * 128, 31u, cap, 0xA00 + gi); acquire_agent(); }
        LBAR();
        const Unit uo{wsr, mk_srd(a.wo, 1024 * 1024 * 2), OB_OFF + (u32)blk * 256 * 2048, (u32)nt * 256, 2048, true};
        gemm_prime(L, uo);
        gemm_tile<true>(L, ws, uo, 0u, false, false, uo, acc);
        u16* op = a.out + (size_t)(blk * 256 + 64 * wm + (lane & 15)) * 2048 + 1024 + nt * 128 + 64 * wn + 4 * (lane >> 4);
#pragma unroll
        for (int m = 0; m < 4; ++m)
#pragma unroll
            for (int n = 0; n < 4; ++n) *(u32x2*)(op + m * 16 * 2048 + n * 16) = u32x2{pk(acc[m][n][0], acc[m][n][1]), pk(acc[m][n][2], acc[m][n][3])};
        if (a.stag & 2) { DRAIN(); LBAR(); if (w == 0) { __builtin_amdgcn_fence(__ATOMIC_RELEASE, "agent"); DRAIN(); } }
    }
}

__global__ void init_kernel(u32x4* p) { p[blockIdx.x * 256 + threadIdx.x] = u32x4{SENT, SENT, SENT, SENT}; }
constexpr int CODE_KB = 48;
__global__ void __launch_bounds__(512, 2) mega_kernel(Args a) {
    extern __shared__ __attribute__((aligned(16))) char smem_[];
    LAS char* L = (LAS char*)smem_;
    const rsrc_t ws = __builtin_amdgcn_make_buffer_rsrc(a.ws, 0, WS_BYTES, 0x00020000);
    const int b = blockIdx.x;
    if ((a.flags & 8192) && threadIdx.x >= 448) {
        const char* pc = (const char*)__builtin_amdgcn_s_getpc();
        u32 sum = 0;
#pragma unroll
        for (int i = 0; i < CODE_KB / 8; ++i) sum += *(const u32*)(pc + (i * 64 + (threadIdx.x & 63)) * 128);
        asm volatile("" :: "v"(sum));
    }
    int role;
    { const int x = b & 7, y = b >> 3; role = x < 4 ? (y >> 3) * 32 + (y & 7) * 4 + x : 128 + (x - 4) + 4 * y; }
    if (role < 128) { if (!(a.flags & 256)) chain_role(a, L, ws, role); }
    else if (role < 160) wo_role(a, L, ws, role - 128);
    else if (!(a.flags & 4)) gemm_role(a, L, ws, role - 160);
}

size_t g_wsoff = 0;
int g_e1 = 54, g_e2 = 72, g_e3 = 72, g_mq0 = 7, g_mup = 2, g_mdn = 12, g_mmax = 16, g_wsl = 0, g_stag = 2, g_wd1 = 9, g_wd2 = 330, g_d3l = 48, g_per = 290, g_flags = 0, g_slack = 1, g_pa = 2;
int g_m1 = 0, g_m2 = 5, g_m2v = 4, g_m3 = 10;
int g_d1 = 26, g_d2 = -6, g_d2v = 89, g_d3 = 64, g_chain_only = 0;
extern "C" void kernel_launch(void* const* d_in, const int* in_sizes, int n_in, void* d_out, int out_size, void* d_ws, size_t ws_size, hipStream_t stream) {
    char* ws = (char*)((((size_t)d_ws + g_wsoff) + 0x1FFFFF) & ~(size_t)0x1FFFFF);
    (void)hipFuncSetAttribute((const void*)mega_kernel, hipFuncAttributeMaxDynamicSharedMemorySize, LDS_BYTES);
    init_kernel<<<12, 256, 0, stream>>>((u32x4*)ws);
    Args a{(const u16*)d_in[0], (const u16*)d_in[1], (const u16*)d_in[2], (const u16*)d_in[3], (const u16*)d_in[4], (const u16*)d_in[5], (const u16*)d_in[7], (const u16*)d_in[8], (const u16*)d_in[9],
           (u16*)d_out, ws, g_d1, g_d2, g_d2v, g_d3, g_m1, g_m2, g_m2v, g_m3, g_flags | (g_chain_only ? 4 : 0), g_slack, g_pa, g_per, g_wd1, g_wd2, g_d3l, g_stag, g_wsl, g_mq0, g_mup, g_mdn, g_mmax, g_e1, g_e2, g_e3};
    mega_kernel<<<256, 512, LDS_BYTES, stream>>>(a);
}
```
